```python
import torch

def _softmax(x, axis=-1):
    return torch.softmax(x, dim=axis)

def _gelu(x):
    return torch.nn.functional.gelu(x, approximate='tanh')

def _silu(x):
    return torch.nn.functional.silu(x)

def _sigmoid(x):
    return torch.sigmoid(x)

def _layernorm(x, axis=-1, eps=1e-05):
    m = x.mean(dim=axis, keepdim=True)
    v = x.var(dim=axis, keepdim=True, unbiased=False)
    return (x - m) / torch.sqrt(v + eps)

def _scatter_add(tgt, idx, src, axis=0):
    return tgt.clone().index_add_(axis, idx, src)

def _segment_sum(src, seg, n):
    out = torch.zeros((n,) + src.shape[1:], dtype=src.dtype)
    return out.index_add_(0, seg, src)

def _pad(x, pw):
    flat = tuple((v for pair in reversed(pw) for v in pair))
    return torch.nn.functional.pad(x, flat)
kernel_name = 'standalone__autoregressive_conv_wavenet__speech'

def setup_inputs():

    _w = lambda a: (lambda t: t.to(torch.bfloat16) if t.is_floating_point() else t)(torch.as_tensor(a))
    B, T, D, K = (2, 16000, 256, 5)
    return {'x': _w((lambda *s: torch.randn(*s, dtype=torch.bfloat16) * 0.1)(B, T, D)), 'w': _w((lambda *s: torch.randn(*s, dtype=torch.bfloat16) * 0.1)(K, D, 2 * D)), 'w_o': _w((lambda *s: torch.randn(*s, dtype=torch.bfloat16) * 0.05)(D, D))}

def reference(x, w, w_o):
    B, T, D = x.shape
    K = w.shape[0]
    dil = 2
    pad = (K - 1) * dil
    xp = _pad(x, ((0, 0), (pad, 0), (0, 0)))
    h = torch.einsum('btc,cd->btd', xp[:, pad:pad + T, :], w[0])
    for i in range(1, K):
        off = pad - i * dil
        h = h + torch.einsum('btc,cd->btd', xp[:, off:off + T, :], w[i])
    f = h[..., :D].clone()
    g = h[..., D:].clone()
    z = torch.tanh(f) * _sigmoid(g)
    return z @ w_o
```

```cpp
#include <hip/hip_runtime.h>
#include <hip/hip_bf16.h>
#include <stdint.h>

typedef __attribute__((ext_vector_type(8))) short bf16x8;
typedef __attribute__((ext_vector_type(4))) float f32x4;
typedef __attribute__((ext_vector_type(4))) unsigned int u32x4;

#define NW 8
constexpr int kT = 16000, kD = 256, kBM = 128;
constexpr int kNT = 32 / NW;
constexpr int kOT = 16 / NW;
constexpr int kXCB = 17 * 1024;
constexpr int kXBytes = 4 * kXCB;
constexpr int kZBytes = 64 * 1024;
constexpr int kLds = kXBytes + kZBytes;
constexpr int kWtBytes = 40 * 32 * 1024;

__global__ __launch_bounds__(256) void prep_kernel(const unsigned short* __restrict__ w,
                                                   const unsigned short* __restrict__ wo,
                                                   u32x4* __restrict__ wt) {
  int gid = blockIdx.x * 256 + threadIdx.x;
  int lane = gid & 63, frag = gid >> 6;
  int q = lane >> 4, rho = lane & 15;
  const unsigned short* src;
  int stride;
  if (frag < 1280) {
    int s = frag >> 5, rem = frag & 31;
    int wv = rem / kNT, tau = rem % kNT;
    int it = s >> 1, ks = s & 1, cb = it / 5, tap = it % 5;
    int half = kNT / 2;
    int tz = tau % half;
    int n = (tau >= half ? 256 : 0) + wv * (half * 16) + 32 * (tz >> 1) + 8 * (rho >> 2) + 4 * (tz & 1) + (rho & 3);
    int c = 64 * cb + 32 * ks + 8 * q;
    src = w + ((size_t)tap * 256 + c) * 512 + n;
    stride = 512;
  } else {
    int f2 = frag - 1280;
    int ks = f2 >> 4, ot = f2 & 15;
    int o = 32 * (ot >> 1) + 8 * (rho >> 2) + 4 * (ot & 1) + (rho & 3);
    src = wo + (size_t)(32 * ks + 8 * q) * 256 + o;
    stride = 256;
  }
  unsigned v[8];
#pragma unroll
  for (int j = 0; j < 8; ++j) v[j] = src[(size_t)j * stride];
  u32x4 r;
  r[0] = v[0] | (v[1] << 16); r[1] = v[2] | (v[3] << 16);
  r[2] = v[4] | (v[5] << 16); r[3] = v[6] | (v[7] << 16);
  wt[gid] = r;
}

__device__ __forceinline__ void glds16(const void* gsrc, unsigned lds_dst) {
  unsigned keep;
  asm volatile("s_mov_b32 %0, m0\n\ts_mov_b32 m0, %2\n\ts_nop 0\n\tglobal_load_lds_dwordx4 %1, off\n\ts_mov_b32 m0, %0"
               : "=&s"(keep) : "v"(gsrc), "s"(lds_dst) : "memory");
}

__device__ __forceinline__ unsigned pk_bf16(float a, float b) {
  typedef __attribute__((ext_vector_type(2))) __bf16 bf2;
  typedef __attribute__((ext_vector_type(2))) float f2;
  f2 v = {a, b};
  bf2 r = __builtin_convertvector(v, bf2);
  return __builtin_bit_cast(unsigned, r);
}

__device__ __forceinline__ float gate(float f, float g) {
  const float kL2E = 1.4426950408889634f;
  float t = __builtin_amdgcn_exp2f(-__builtin_fabsf(f * (2.0f * kL2E)));
  float s = __builtin_amdgcn_exp2f(-(g * kL2E));
  float b = 1.0f + t;
  float d = __builtin_fmaf(b, s, b);
  float z = (1.0f - t) * __builtin_amdgcn_rcpf(d);
  return __builtin_copysignf(z, f);
}

__device__ __forceinline__ int xpos(int row, int chunk) {
  int rho = row & 7;
  return (row >> 3) * 1024 + ((chunk >> 2) * 32 + (rho >> 2) * 16 + (rho & 3) * 4 + ((chunk & 3) ^ ((rho >> 2) << 1))) * 16;
}

__global__ __launch_bounds__(NW * 64, NW / 4) void wavenet_kernel(const unsigned short* __restrict__ x,
                                                                   const u32x4* __restrict__ wt,
                                                                   unsigned short* __restrict__ out) {
  extern __shared__ __attribute__((aligned(1024))) char smem[];
  const int tid = threadIdx.x, lane = tid & 63;
  const int wave = __builtin_amdgcn_readfirstlane(tid >> 6);
  const int q = lane >> 4, ml = lane & 15;
  const int tile = blockIdx.x;
  const int r0 = tile * kBM;
  const bool first = (r0 % kT) == 0;
  const unsigned lds0 = (unsigned)(uintptr_t)smem;

  {
    int rho = ((lane >> 4) & 1) * 4 + ((lane >> 2) & 3);
    int chunk = (lane >> 5) * 4 + ((lane & 3) ^ (((lane >> 4) & 1) << 1));
    const char* xs = (const char*)x + (size_t)(r0 - 8 + rho) * 512 + chunk * 16;
    for (int p = wave; p < 68; p += NW) {
      int cb = p / 17, rb = p % 17;
      if (rb == 0 && first) {
        *(u32x4*)(smem + p * 1024 + lane * 16) = (u32x4){0, 0, 0, 0};
      } else {
        glds16(xs + (size_t)rb * 4096 + cb * 128, lds0 + p * 1024);
      }
    }
  }
  const u32x4* wp = wt + wave * (kNT * 64) + lane;
  bf16x8 wa[kNT], wb[kNT];
#pragma unroll
  for (int t = 0; t < kNT; ++t) wa[t] = __builtin_bit_cast(bf16x8, wp[t * 64]);
#pragma unroll
  for (int t = 0; t < kNT; ++t) wb[t] = __builtin_bit_cast(bf16x8, wp[2048 + t * 64]);
  wp += 4096;

  f32x4 acc[kNT][8];
#pragma unroll
  for (int t = 0; t < kNT; ++t)
#pragma unroll
    for (int m = 0; m < 8; ++m) acc[t][m] = (f32x4){0.f, 0.f, 0.f, 0.f};

  asm volatile("s_waitcnt vmcnt(0) lgkmcnt(0)" ::: "memory");
  __builtin_amdgcn_s_barrier();

  int tap = 0, cbo = 0;
  for (int it = 0; it < 20; ++it) {
    const char* xb = smem + cbo + xpos(ml + 8 - 2 * tap, q);
    bf16x8 xf[8];
#pragma unroll
    for (int m = 0; m < 8; ++m) xf[m] = *(const bf16x8*)(xb + m * 2048);
#pragma unroll
    for (int t = 0; t < kNT; ++t) {
#pragma unroll
      for (int m = 0; m < 8; ++m)
        acc[t][m] = __builtin_amdgcn_mfma_f32_16x16x32_bf16(wa[t], xf[m], acc[t][m], 0, 0, 0);
      wa[t] = __builtin_bit_cast(bf16x8, wp[t * 64]);
    }
#pragma unroll
    for (int m = 0; m < 8; ++m) xf[m] = *(const bf16x8*)(xb + m * 2048 + 512);
#pragma unroll
    for (int t = 0; t < kNT; ++t) {
#pragma unroll
      for (int m = 0; m < 8; ++m)
        acc[t][m] = __builtin_amdgcn_mfma_f32_16x16x32_bf16(wb[t], xf[m], acc[t][m], 0, 0, 0);
      wb[t] = __builtin_bit_cast(bf16x8, wp[2048 + t * 64]);
    }
    wp += 4096;
    ++tap;
    if (tap == 5) { tap = 0; cbo += kXCB; }
  }

  const u32x4* wop = wt + 1280 * 64 + wave * (kOT * 64) + lane;
  bf16x8 wo[8][kOT];
#pragma unroll
  for (int k = 0; k < 8; ++k)
#pragma unroll
    for (int t = 0; t < kOT; ++t) wo[k][t] = __builtin_bit_cast(bf16x8, wop[k * 1024 + t * 64]);

  char* zb = smem + kXBytes;
  constexpr int H = kNT / 2;
#pragma unroll
  for (int u = 0; u < H / 2; ++u) {
#pragma unroll
    for (int m = 0; m < 8; ++m) {
      f32x4 f0 = acc[2 * u][m], f1 = acc[2 * u + 1][m], g0 = acc[H + 2 * u][m], g1 = acc[H + 2 * u + 1][m];
      u32x4 zz;
      zz[0] = pk_bf16(gate(f0[0], g0[0]), gate(f0[1], g0[1]));
      zz[1] = pk_bf16(gate(f0[2], g0[2]), gate(f0[3], g0[3]));
      zz[2] = pk_bf16(gate(f1[0], g1[0]), gate(f1[1], g1[1]));
      zz[3] = pk_bf16(gate(f1[2], g1[2]), gate(f1[3], g1[3]));
      *(u32x4*)(zb + ((wave * (H / 2) + u) * 8 + m) * 1024 + lane * 16) = zz;
    }
  }
  __syncthreads();

  f32x4 oacc[kOT][8];
#pragma unroll
  for (int t = 0; t < kOT; ++t)
#pragma unroll
    for (int m = 0; m < 8; ++m) oacc[t][m] = (f32x4){0.f, 0.f, 0.f, 0.f};
#pragma unroll
  for (int k = 0; k < 8; ++k) {
#pragma unroll
    for (int m = 0; m < 8; ++m) {
      bf16x8 zf = *(const bf16x8*)(zb + (k * 8 + m) * 1024 + lane * 16);
#pragma unroll
      for (int t = 0; t < kOT; ++t)
        oacc[t][m] = __builtin_amdgcn_mfma_f32_16x16x32_bf16(wo[k][t], zf, oacc[t][m], 0, 0, 0);
    }
  }
#pragma unroll
  for (int m = 0; m < 8; ++m) {
#pragma unroll
    for (int u = 0; u < kOT / 2; ++u) {
      f32x4 a = oacc[2 * u][m], b = oacc[2 * u + 1][m];
      u32x4 o;
      o[0] = pk_bf16(a[0], a[1]); o[1] = pk_bf16(a[2], a[3]);
      o[2] = pk_bf16(b[0], b[1]); o[3] = pk_bf16(b[2], b[3]);
      *(u32x4*)(out + (size_t)(r0 + 16 * m + ml) * 256 + wave * (kOT * 16) + 32 * u + 8 * q) = o;
    }
  }
}

extern "C" void kernel_launch(void* const* d_in, const int* in_sizes, int n_in, void* d_out, int out_size,
                              void* d_ws, size_t ws_size, hipStream_t stream) {
  const unsigned short* x = (const unsigned short*)d_in[0];
  const unsigned short* w = (const unsigned short*)d_in[1];
  const unsigned short* wo = (const unsigned short*)d_in[2];
  u32x4* wt = (u32x4*)d_ws;
  int rows = in_sizes[0] / kD;
  (void)hipFuncSetAttribute((const void*)wavenet_kernel, hipFuncAttributeMaxDynamicSharedMemorySize, kLds);
  prep_kernel<<<(1280 + 128) * 64 / 256, 256, 0, stream>>>(w, wo, wt);
  wavenet_kernel<<<rows / kBM, NW * 64, kLds, stream>>>(x, wt, (unsigned short*)d_out);
}
```

```cpp
#include <hip/hip_runtime.h>
#include <stdint.h>

constexpr int kT = 16000, kD = 256, kBM = 128;
constexpr int kRB = 4 * 17 * 1024;
constexpr int kRing = 20480;
constexpr int kLds = 151552;

__device__ __forceinline__ unsigned xpos(int row, int q) {
  int r2 = (row >> 2) & 1;
  return (row >> 3) * 1024 + (r2 * 16 + (row & 3) * 4 + (q ^ (r2 << 1))) * 16;
}

__global__ __launch_bounds__(256, 1) void wavenet_kernel(const char* __restrict__ x, const char* __restrict__ w,
                                                         const char* __restrict__ wo, char* __restrict__ out,
                                                         char* ws, unsigned xbytes) {
  __shared__ __attribute__((aligned(1024))) char smem[kLds];
  int tid = threadIdx.x;
  int wave = __builtin_amdgcn_readfirstlane(tid >> 6);
  int tile = blockIdx.x;
  const unsigned lds0 = (unsigned)(uintptr_t)smem;
  const int rot = (tile >> 3) % 5;
  int tp[5];
#pragma unroll
  for (int i = 0; i < 5; ++i) tp[i] = rot + i >= 5 ? rot + i - 5 : rot + i;
  unsigned cb0 = 0u;
  unsigned vX, vWd;
  {
    const int lane = tid & 63, h = lane >> 5, q = lane >> 4;
    const int r2 = q & 1, a2 = (lane >> 2) & 3, p2 = lane & 3, u2 = (a2 >> 1) ^ r2;
    vX = (r2 * 4 + a2) * 512 + (h * 4 + (p2 ^ (r2 << 1))) * 16;
    vWd = (2 * q + u2) * 4096 + (a2 & 1) * 512 + p2 * 16 + wave * 64;
  }
  const bool first = (tile * kBM % kT) == 0;
  const unsigned sxb = (unsigned)(tile * kBM - 8 + 8 * wave) * 512u;
  const unsigned sx0 = (first && wave == 0) ? 0x7f000000u : sxb;
  const unsigned sx1 = sxb + 16384u;
  const unsigned sx4 = (unsigned)(tile * kBM + 120) * 512u;
  const unsigned sm0 = lds0 + wave * 1024, sm4 = lds0 + 16384;
  const unsigned ring = lds0 + kRB + wave * kRing;
  asm volatile(
"s_mov_b32 m0, %[sm0]\n"
"s_mov_b64 s[40:41], %[px]\n"
"s_mov_b32 s42, %[xbytes]\n"
"s_mov_b32 s43, 0x20000\n"
"buffer_load_dwordx4 %[vX], s[40:43], %[sx0] offen lds\n"
"s_add_u32 m0, %[sm0], 4096\n"
"s_add_u32 s57, %[sx1], 0\n"
"buffer_load_dwordx4 %[vX], s[40:43], s57 offen lds\n"
"s_add_u32 m0, %[sm0], 8192\n"
"s_add_u32 s57, %[sx1], 16384\n"
"buffer_load_dwordx4 %[vX], s[40:43], s57 offen lds\n"
"s_add_u32 m0, %[sm0], 12288\n"
"s_add_u32 s57, %[sx1], 32768\n"
"buffer_load_dwordx4 %[vX], s[40:43], s57 offen lds\n"
"s_mov_b32 m0, %[sm4]\n"
"s_mov_b32 s53, 0\n"
"buffer_load_dwordx4 %[vX], s[40:43], %[sx4] offen lds\n"
"s_mov_b64 s[44:45], %[pw]\n"
"s_mov_b32 s46, 1310720\n"
"s_mov_b32 s47, 0x20000\n"
"s_mov_b32 s62, %[ring]\n"
"s_mov_b32 s48, %[wof0]\n"
"s_mov_b32 s49, %[wof1]\n"
"s_mov_b32 s50, %[wof2]\n"
"v_xor_b32 v29, 0, %[vWd]\n"
"v_xor_b32 v30, 32, %[vWd]\n"
"v_xor_b32 v31, 512, %[vWd]\n"
"v_xor_b32 v32, 544, %[vWd]\n"
"s_add_u32 m0, s62, 0\n"
"s_add_u32 s61, s48, s53\n"
"buffer_load_dwordx4 v29, s[44:47], s61 offen offset:0 lds\n"
"buffer_load_dwordx4 v30, s[44:47], s61 offen offset:1024 lds\n"
"buffer_load_dwordx4 v31, s[44:47], s61 offen offset:2048 lds\n"
"buffer_load_dwordx4 v32, s[44:47], s61 offen offset:3072 lds\n"
      : "+v"(tid), "+s"(wave), "+s"(tile)
      : [px] "s"(x), [xbytes] "s"(xbytes), [sx0] "s"(sx0), [sx1] "s"(sx1), [sx4] "s"(sx4), [sm0] "s"(sm0), [sm4] "s"(sm4),
        [pw] "s"(w), [ring] "s"(ring),
        [wof0] "s"(tp[0] * 262144), [wof1] "s"(tp[1] * 262144), [wof2] "s"(tp[2] * 262144), [vX] "v"(vX), [vWd] "v"(vWd)
      : "v29", "v30", "v31", "v32", "v41", "s39", "s40", "s41", "s42", "s43", "s44", "s45", "s46", "s47", "s48", "s49", "s50", "s51", "s52", "s53", "s54", "s55", "s56", "s57", "s58", "s59", "s60", "s61", "s62", "s63", "s64", "s65", "s66", "s67", "s68", "s69", "s70", "s71", "s72", "s73", "s74", "s75", "s76", "s77", "s78", "s79", "s80", "s81", "s82", "s83", "s84", "s85", "s86", "s87", "s88", "s89", "s90", "s91", "s92", "s93", "s94", "s95", "s96", "s97", "s98", "s99", "scc", "memory");
  const int lane = tid & 63, h = lane >> 5, t = lane & 31;
  const int q = lane >> 4, ml = lane & 15;
  const int r2 = q & 1, a2 = (lane >> 2) & 3, p2 = lane & 3;
  const int u2 = (a2 >> 1) ^ r2;
  const unsigned vTA = ring + a2 * 1024 + (16 * q + 8 * r2 + 2 * a2) * 16 + p2 * 8;
  const unsigned vTO = lds0 + wave * 4096 + a2 * 1024 + (24 * h + ((2 * r2) ^ (2 * a2)) + (p2 & 1)) * 16 + (p2 >> 1) * 8;
  const unsigned vWo = (16 * u2 + 4 * q) * 512 + (lane & 7) * 16 + wave * 128;
  const unsigned vZr = lds0 + kRB + r2 * 1024 + h * 256 + ml * 16;
  const unsigned vZw = ring + lane * 16;
  const unsigned vO = (lane >> 3) * 512 + wave * 128 + ((lane ^ (lane >> 3)) & 7) * 16 + 4096;
  const unsigned vOw = ring + t * 128 + ((h ^ t) & 7) * 16;
  const char* pout = out + (size_t)tile * kBM * 512;
  const unsigned spf = ((tile >> 3) & 7) * 4096 + wave * 1024;
  asm volatile(
"s_mov_b32 s53, 0\n"
"s_mov_b64 s[44:45], %[pw]\n"
"s_mov_b32 s46, 1310720\n"
"s_mov_b32 s47, 0x20000\n"
"s_mov_b32 s62, %[ring]\n"
"s_mov_b32 s48, %[wof0]\n"
"s_mov_b32 s49, %[wof1]\n"
"s_mov_b32 s50, %[wof2]\n"
"s_mov_b32 s51, %[wof3]\n"
"s_mov_b32 s52, %[wof4]\n"
"v_xor_b32 v29, 0, %[vWd]\n"
"v_xor_b32 v30, 32, %[vWd]\n"
"v_xor_b32 v31, 512, %[vWd]\n"
"v_xor_b32 v32, 544, %[vWd]\n"
"s_add_u32 s61, s48, s53\n"
"s_add_u32 m0, s62, 4096\n"
"s_add_u32 s61, s61, 0x8000\n"
"buffer_load_dwordx4 v29, s[44:47], s61 offen offset:0 lds\n"
"s_mov_b64 s[40:41], %[px]\n"
"s_mov_b32 s42, %[xbytes]\n"
"s_mov_b32 s43, 0x20000\n"
"s_mov_b32 s53, %[cb0]\n"
"s_lshr_b32 s54, s53, 9\n"
"v_add_u32 v41, s54, %[vX]\n"
"s_mov_b32 s56, %[sx0]\n"
"s_add_u32 s57, %[sx1], 0\n"
"s_add_u32 s58, %[sx1], 16384\n"
"s_add_u32 s59, %[sx1], 32768\n"
"s_mov_b32 s60, %[sx4]\n"
"s_add_u32 s88, %[sm0], 0\n"
"buffer_load_dwordx4 v30, s[44:47], s61 offen offset:1024 lds\n"
"s_add_u32 s89, %[sm0], 4096\n"
"s_add_u32 s90, %[sm0], 8192\n"
"s_add_u32 s91, %[sm0], 12288\n"
"s_mov_b32 s92, %[sm4]\n"
"s_getpc_b64 s[94:95]\n"
".Lwn_pf:\n"
"v_subrev_u32 v42, s62, %[vZw]\n"
"s_and_b32 s99, %[spf], 0xc00\n"
"s_add_u32 s99, s99, .Lwn_loop-.Lwn_pf\n"
"v_add_u32 v43, s99, v42\n"
"global_load_dwordx4 v[48:51], v43, s[94:95]\n"
"s_mov_b32 s93, %[spf]\n"
"buffer_load_dwordx4 v31, s[44:47], s61 offen offset:2048 lds\n"
"v_add_u32 v42, .Lwn_p2-.Lwn_pf, v42\n"
"s_mov_b32 s55, 0\n"
"s_mov_b32 s71, 17408\n"
"s_mov_b64 s[64:65], %[pwo]\n"
"s_sub_u32 s64, s64, 2048\n"
"s_subb_u32 s65, s65, 0\n"
"s_mov_b32 s66, 133120\n"
"s_mov_b32 s67, 0x20000\n"
"s_mov_b64 s[96:97], %[pout]\n"
"s_and_b32 s98, s53, 256\n"
"s_lshl_b32 s98, s98, 8\n"
"s_mov_b32 s68, 0x4038aa3b\n"
"buffer_load_dwordx4 v32, s[44:47], s61 offen offset:3072 lds\n"
"s_mov_b32 s69, 0x3fb8aa3b\n"
"s_mov_b32 s70, 0x7fffffff\n"
"v_mov_b32 v16, %[xa0]\n"
"v_mov_b32 v17, %[xa1]\n"
"v_mov_b32 v18, %[xa2]\n"
"v_mov_b32 v19, %[xa3]\n"
"v_mov_b32 v20, %[xa4]\n"
"v_xor_b32 v21, 0, %[vTA]\n"
"v_xor_b32 v22, 32, %[vTA]\n"
"v_xor_b32 v23, 64, %[vTA]\n"
"v_xor_b32 v24, 96, %[vTA]\n"
"v_xor_b32 v37, 0, %[vTO]\n"
"s_add_u32 m0, s62, 8192\n"
"s_add_u32 s61, s49, s53\n"
"buffer_load_dwordx4 v29, s[44:47], s61 offen offset:0 lds\n"
"v_xor_b32 v38, 64, %[vTO]\n"
"v_xor_b32 v25, 128, %[vTA]\n"
"v_xor_b32 v26, 160, %[vTA]\n"
"v_xor_b32 v27, 192, %[vTA]\n"
"v_xor_b32 v28, 224, %[vTA]\n"
"v_xor_b32 v39, 128, %[vTO]\n"
"v_xor_b32 v40, 192, %[vTO]\n"
"v_xor_b32 v33, 0, %[vWo]\n"
"v_add_u32 v33, 2048, v33\n"
"v_xor_b32 v34, 32, %[vWo]\n"
"v_add_u32 v34, 1536, v34\n"
"v_xor_b32 v35, 64, %[vWo]\n"
"buffer_load_dwordx4 v30, s[44:47], s61 offen offset:1024 lds\n"
"v_add_u32 v35, 1024, v35\n"
"v_xor_b32 v36, 96, %[vWo]\n"
"v_add_u32 v36, 512, v36\n"
"v_accvgpr_write_b32 a0, 0\n"
"v_accvgpr_write_b32 a1, 0\n"
"v_accvgpr_write_b32 a2, 0\n"
"v_accvgpr_write_b32 a3, 0\n"
"v_accvgpr_write_b32 a4, 0\n"
"v_accvgpr_write_b32 a5, 0\n"
"v_accvgpr_write_b32 a6, 0\n"
"v_accvgpr_write_b32 a7, 0\n"
"v_accvgpr_write_b32 a8, 0\n"
"buffer_load_dwordx4 v31, s[44:47], s61 offen offset:2048 lds\n"
"v_accvgpr_write_b32 a9, 0\n"
"v_accvgpr_write_b32 a10, 0\n"
"v_accvgpr_write_b32 a11, 0\n"
"v_accvgpr_write_b32 a12, 0\n"
"v_accvgpr_write_b32 a13, 0\n"
"v_accvgpr_write_b32 a14, 0\n"
"v_accvgpr_write_b32 a15, 0\n"
"v_accvgpr_write_b32 a16, 0\n"
"v_accvgpr_write_b32 a17, 0\n"
"v_accvgpr_write_b32 a18, 0\n"
"v_accvgpr_write_b32 a19, 0\n"
"v_accvgpr_write_b32 a20, 0\n"
"buffer_load_dwordx4 v32, s[44:47], s61 offen offset:3072 lds\n"
"v_accvgpr_write_b32 a21, 0\n"
"v_accvgpr_write_b32 a22, 0\n"
"v_accvgpr_write_b32 a23, 0\n"
"v_accvgpr_write_b32 a24, 0\n"
"v_accvgpr_write_b32 a25, 0\n"
"v_accvgpr_write_b32 a26, 0\n"
"v_accvgpr_write_b32 a27, 0\n"
"v_accvgpr_write_b32 a28, 0\n"
"v_accvgpr_write_b32 a29, 0\n"
"v_accvgpr_write_b32 a30, 0\n"
"v_accvgpr_write_b32 a31, 0\n"
"v_accvgpr_write_b32 a32, 0\n"
"s_add_u32 m0, s62, 12288\n"
"s_add_u32 s61, s61, 0x8000\n"
"buffer_load_dwordx4 v29, s[44:47], s61 offen offset:0 lds\n"
"v_accvgpr_write_b32 a33, 0\n"
"v_accvgpr_write_b32 a34, 0\n"
"v_accvgpr_write_b32 a35, 0\n"
"v_accvgpr_write_b32 a36, 0\n"
"v_accvgpr_write_b32 a37, 0\n"
"v_accvgpr_write_b32 a38, 0\n"
"v_accvgpr_write_b32 a39, 0\n"
"v_accvgpr_write_b32 a40, 0\n"
"v_accvgpr_write_b32 a41, 0\n"
"v_accvgpr_write_b32 a42, 0\n"
"v_accvgpr_write_b32 a43, 0\n"
"v_accvgpr_write_b32 a44, 0\n"
"buffer_load_dwordx4 v30, s[44:47], s61 offen offset:1024 lds\n"
"v_accvgpr_write_b32 a45, 0\n"
"v_accvgpr_write_b32 a46, 0\n"
"v_accvgpr_write_b32 a47, 0\n"
"v_accvgpr_write_b32 a48, 0\n"
"v_accvgpr_write_b32 a49, 0\n"
"v_accvgpr_write_b32 a50, 0\n"
"v_accvgpr_write_b32 a51, 0\n"
"v_accvgpr_write_b32 a52, 0\n"
"v_accvgpr_write_b32 a53, 0\n"
"v_accvgpr_write_b32 a54, 0\n"
"v_accvgpr_write_b32 a55, 0\n"
"v_accvgpr_write_b32 a56, 0\n"
"buffer_load_dwordx4 v31, s[44:47], s61 offen offset:2048 lds\n"
"v_accvgpr_write_b32 a57, 0\n"
"v_accvgpr_write_b32 a58, 0\n"
"v_accvgpr_write_b32 a59, 0\n"
"v_accvgpr_write_b32 a60, 0\n"
"v_accvgpr_write_b32 a61, 0\n"
"v_accvgpr_write_b32 a62, 0\n"
"v_accvgpr_write_b32 a63, 0\n"
"v_accvgpr_write_b32 a64, 0\n"
"v_accvgpr_write_b32 a65, 0\n"
"v_accvgpr_write_b32 a66, 0\n"
"v_accvgpr_write_b32 a67, 0\n"
"v_accvgpr_write_b32 a68, 0\n"
"buffer_load_dwordx4 v32, s[44:47], s61 offen offset:3072 lds\n"
"v_accvgpr_write_b32 a69, 0\n"
"v_accvgpr_write_b32 a70, 0\n"
"v_accvgpr_write_b32 a71, 0\n"
"v_accvgpr_write_b32 a72, 0\n"
"v_accvgpr_write_b32 a73, 0\n"
"v_accvgpr_write_b32 a74, 0\n"
"v_accvgpr_write_b32 a75, 0\n"
"v_accvgpr_write_b32 a76, 0\n"
"v_accvgpr_write_b32 a77, 0\n"
"v_accvgpr_write_b32 a78, 0\n"
"v_accvgpr_write_b32 a79, 0\n"
"v_accvgpr_write_b32 a80, 0\n"
"s_add_u32 m0, s62, 16384\n"
"s_add_u32 s61, s50, s53\n"
"buffer_load_dwordx4 v29, s[44:47], s61 offen offset:0 lds\n"
"v_accvgpr_write_b32 a81, 0\n"
"v_accvgpr_write_b32 a82, 0\n"
"v_accvgpr_write_b32 a83, 0\n"
"v_accvgpr_write_b32 a84, 0\n"
"v_accvgpr_write_b32 a85, 0\n"
"v_accvgpr_write_b32 a86, 0\n"
"v_accvgpr_write_b32 a87, 0\n"
"v_accvgpr_write_b32 a88, 0\n"
"v_accvgpr_write_b32 a89, 0\n"
"v_accvgpr_write_b32 a90, 0\n"
"v_accvgpr_write_b32 a91, 0\n"
"v_accvgpr_write_b32 a92, 0\n"
"buffer_load_dwordx4 v30, s[44:47], s61 offen offset:1024 lds\n"
"v_accvgpr_write_b32 a93, 0\n"
"v_accvgpr_write_b32 a94, 0\n"
"v_accvgpr_write_b32 a95, 0\n"
"v_accvgpr_write_b32 a96, 0\n"
"v_accvgpr_write_b32 a97, 0\n"
"v_accvgpr_write_b32 a98, 0\n"
"v_accvgpr_write_b32 a99, 0\n"
"v_accvgpr_write_b32 a100, 0\n"
"v_accvgpr_write_b32 a101, 0\n"
"v_accvgpr_write_b32 a102, 0\n"
"v_accvgpr_write_b32 a103, 0\n"
"v_accvgpr_write_b32 a104, 0\n"
"buffer_load_dwordx4 v31, s[44:47], s61 offen offset:2048 lds\n"
"v_accvgpr_write_b32 a105, 0\n"
"v_accvgpr_write_b32 a106, 0\n"
"v_accvgpr_write_b32 a107, 0\n"
"v_accvgpr_write_b32 a108, 0\n"
"v_accvgpr_write_b32 a109, 0\n"
"v_accvgpr_write_b32 a110, 0\n"
"v_accvgpr_write_b32 a111, 0\n"
"v_accvgpr_write_b32 a112, 0\n"
"v_accvgpr_write_b32 a113, 0\n"
"v_accvgpr_write_b32 a114, 0\n"
"v_accvgpr_write_b32 a115, 0\n"
"v_accvgpr_write_b32 a116, 0\n"
"buffer_load_dwordx4 v32, s[44:47], s61 offen offset:3072 lds\n"
"v_accvgpr_write_b32 a117, 0\n"
"v_accvgpr_write_b32 a118, 0\n"
"v_accvgpr_write_b32 a119, 0\n"
"v_accvgpr_write_b32 a120, 0\n"
"v_accvgpr_write_b32 a121, 0\n"
"v_accvgpr_write_b32 a122, 0\n"
"v_accvgpr_write_b32 a123, 0\n"
"v_accvgpr_write_b32 a124, 0\n"
"v_accvgpr_write_b32 a125, 0\n"
"v_accvgpr_write_b32 a126, 0\n"
"v_accvgpr_write_b32 a127, 0\n"
"s_add_u32 m0, s62, 0\n"
"s_add_u32 s61, s61, 0x8000\n"
"s_waitcnt vmcnt(17)\n"
"s_barrier\n"
"ds_read_b128 a[128:131], v16 offset:0\n"
"ds_read_b128 a[132:135], v16 offset:2048\n"
"ds_read_b128 a[136:139], v16 offset:4096\n"
"ds_read_b128 a[140:143], v16 offset:6144\n"
"ds_read_b128 a[144:147], v16 offset:8192\n"
"ds_read_b128 a[148:151], v16 offset:10240\n"
"ds_read_b128 a[152:155], v16 offset:12288\n"
"ds_read_b128 a[156:159], v16 offset:14336\n"
"ds_read_b64_tr_b16 a[192:193], v21 offset:0\n"
"ds_read_b64_tr_b16 a[194:195], v25 offset:0\n"
"ds_read_b64_tr_b16 a[196:197], v22 offset:0\n"
"ds_read_b64_tr_b16 a[198:199], v26 offset:0\n"
"ds_read_b64_tr_b16 a[200:201], v23 offset:0\n"
"ds_read_b64_tr_b16 a[202:203], v27 offset:0\n"
"ds_read_b64_tr_b16 a[204:205], v24 offset:0\n"
"ds_read_b64_tr_b16 a[206:207], v28 offset:0\n"
"s_waitcnt lgkmcnt(4)\n"
".p2align 6\n"
".Lwn_loop:\n"
"v_mfma_f32_16x16x32_bf16 a[0:3], a[192:195], a[128:131], a[0:3]\n"
"ds_read_b128 a[160:163], v16 offset:512\n"
"v_mfma_f32_16x16x32_bf16 a[16:19], a[192:195], a[132:135], a[16:19]\n"
"ds_read_b128 a[164:167], v16 offset:2560\n"
"v_mfma_f32_16x16x32_bf16 a[32:35], a[192:195], a[136:139], a[32:35]\n"
"s_add_u32 s54, s53, 0x10000\n"
"s_and_b32 s54, s54, 0x3ffff\n"
"v_mfma_f32_16x16x32_bf16 a[48:51], a[192:195], a[140:143], a[48:51]\n"
"ds_read_b128 a[168:171], v16 offset:4608\n"
"v_mfma_f32_16x16x32_bf16 a[64:67], a[192:195], a[144:147], a[64:67]\n"
"ds_read_b128 a[172:175], v16 offset:6656\n"
"v_mfma_f32_16x16x32_bf16 a[80:83], a[192:195], a[148:151], a[80:83]\n"
"s_waitcnt lgkmcnt(4)\n"
"v_mfma_f32_16x16x32_bf16 a[96:99], a[192:195], a[152:155], a[96:99]\n"
"buffer_load_dwordx4 v29, s[44:47], s61 offen offset:0 lds\n"
"v_mfma_f32_16x16x32_bf16 a[112:115], a[192:195], a[156:159], a[112:115]\n"
"ds_read_b128 a[176:179], v16 offset:8704\n"
"v_mfma_f32_16x16x32_bf16 a[4:7], a[196:199], a[128:131], a[4:7]\n"
"s_lshr_b32 s99, s54, 9\n"
"s_cmp_lt_u32 s55, 3\n"
"v_mfma_f32_16x16x32_bf16 a[20:23], a[196:199], a[132:135], a[20:23]\n"
"buffer_load_dwordx4 v30, s[44:47], s61 offen offset:1024 lds\n"
"v_add_u32 v41, s99, %[vX]\n"
"v_mfma_f32_16x16x32_bf16 a[36:39], a[196:199], a[136:139], a[36:39]\n"
"ds_read_b128 a[180:183], v16 offset:10752\n"
"v_mfma_f32_16x16x32_bf16 a[52:55], a[196:199], a[140:143], a[52:55]\n"
"s_cselect_b64 vcc, -1, 0\n"
"s_cselect_b32 s39, 0, 256\n"
"v_mfma_f32_16x16x32_bf16 a[68:71], a[196:199], a[144:147], a[68:71]\n"
"buffer_load_dwordx4 v31, s[44:47], s61 offen offset:2048 lds\n"
"v_mfma_f32_16x16x32_bf16 a[84:87], a[196:199], a[148:151], a[84:87]\n"
"ds_read_b128 a[184:187], v16 offset:12800\n"
"v_mfma_f32_16x16x32_bf16 a[100:103], a[196:199], a[152:155], a[100:103]\n"
"s_mul_i32 s63, s39, -272\n"
"s_add_u32 s63, s63, 17408\n"
"v_mfma_f32_16x16x32_bf16 a[116:119], a[196:199], a[156:159], a[116:119]\n"
"buffer_load_dwordx4 v32, s[44:47], s61 offen offset:3072 lds\n"
"v_mfma_f32_16x16x32_bf16 a[8:11], a[200:203], a[128:131], a[8:11]\n"
"ds_read_b128 a[188:191], v16 offset:14848\n"
"v_mfma_f32_16x16x32_bf16 a[24:27], a[200:203], a[132:135], a[24:27]\n"
"s_waitcnt vmcnt(16)\n"
"v_mfma_f32_16x16x32_bf16 a[40:43], a[200:203], a[136:139], a[40:43]\n"
"ds_read_b64_tr_b16 a[208:209], v21 offset:4096\n"
"v_add_u32 v16, s63, v16\n"
"v_mfma_f32_16x16x32_bf16 a[56:59], a[200:203], a[140:143], a[56:59]\n"
"ds_read_b64_tr_b16 a[210:211], v25 offset:4096\n"
"s_add_u32 m0, s88, s71\n"
"v_mfma_f32_16x16x32_bf16 a[72:75], a[200:203], a[144:147], a[72:75]\n"
"s_cbranch_vccz .Lwn_xs0\n"
"buffer_load_dwordx4 v41, s[40:43], s56 offen lds\n"
".Lwn_xs0:\n"
"v_mfma_f32_16x16x32_bf16 a[88:91], a[200:203], a[148:151], a[88:91]\n"
"ds_read_b64_tr_b16 a[212:213], v22 offset:4096\n"
"v_mfma_f32_16x16x32_bf16 a[104:107], a[200:203], a[152:155], a[104:107]\n"
"ds_read_b64_tr_b16 a[214:215], v26 offset:4096\n"
"v_mfma_f32_16x16x32_bf16 a[120:123], a[200:203], a[156:159], a[120:123]\n"
"v_mfma_f32_16x16x32_bf16 a[12:15], a[204:207], a[128:131], a[12:15]\n"
"ds_read_b64_tr_b16 a[216:217], v23 offset:4096\n"
"v_mfma_f32_16x16x32_bf16 a[28:31], a[204:207], a[132:135], a[28:31]\n"
"ds_read_b64_tr_b16 a[218:219], v27 offset:4096\n"
"s_add_u32 m0, s89, s71\n"
"v_mfma_f32_16x16x32_bf16 a[44:47], a[204:207], a[136:139], a[44:47]\n"
"s_cbranch_vccz .Lwn_xs1\n"
"buffer_load_dwordx4 v41, s[40:43], s57 offen lds\n"
".Lwn_xs1:\n"
"v_mfma_f32_16x16x32_bf16 a[60:63], a[204:207], a[140:143], a[60:63]\n"
"ds_read_b64_tr_b16 a[220:221], v24 offset:4096\n"
"v_mfma_f32_16x16x32_bf16 a[76:79], a[204:207], a[144:147], a[76:79]\n"
"ds_read_b64_tr_b16 a[222:223], v28 offset:4096\n"
"v_mfma_f32_16x16x32_bf16 a[92:95], a[204:207], a[148:151], a[92:95]\n"
"v_mfma_f32_16x16x32_bf16 a[108:111], a[204:207], a[152:155], a[108:111]\n"
"s_add_u32 m0, s62, 4096\n"
"s_add_u32 s61, s51, s53\n"
"v_mfma_f32_16x16x32_bf16 a[124:127], a[204:207], a[156:159], a[124:127]\n"
"s_waitcnt lgkmcnt(4)\n"
"v_mfma_f32_16x16x32_bf16 a[0:3], a[208:211], a[160:163], a[0:3]\n"
"ds_read_b128 a[128:131], v17 offset:0\n"
"v_mfma_f32_16x16x32_bf16 a[16:19], a[208:211], a[164:167], a[16:19]\n"
"ds_read_b128 a[132:135], v17 offset:2048\n"
"v_mfma_f32_16x16x32_bf16 a[32:35], a[208:211], a[168:171], a[32:35]\n"
"v_mfma_f32_16x16x32_bf16 a[48:51], a[208:211], a[172:175], a[48:51]\n"
"ds_read_b128 a[136:139], v17 offset:4096\n"
"v_mfma_f32_16x16x32_bf16 a[64:67], a[208:211], a[176:179], a[64:67]\n"
"ds_read_b128 a[140:143], v17 offset:6144\n"
"v_mfma_f32_16x16x32_bf16 a[80:83], a[208:211], a[180:183], a[80:83]\n"
"s_waitcnt lgkmcnt(4)\n"
"v_mfma_f32_16x16x32_bf16 a[96:99], a[208:211], a[184:187], a[96:99]\n"
"buffer_load_dwordx4 v29, s[44:47], s61 offen offset:0 lds\n"
"v_mfma_f32_16x16x32_bf16 a[112:115], a[208:211], a[188:191], a[112:115]\n"
"ds_read_b128 a[144:147], v17 offset:8192\n"
"v_mfma_f32_16x16x32_bf16 a[4:7], a[212:215], a[160:163], a[4:7]\n"
"v_mfma_f32_16x16x32_bf16 a[20:23], a[212:215], a[164:167], a[20:23]\n"
"buffer_load_dwordx4 v30, s[44:47], s61 offen offset:1024 lds\n"
"v_mfma_f32_16x16x32_bf16 a[36:39], a[212:215], a[168:171], a[36:39]\n"
"ds_read_b128 a[148:151], v17 offset:10240\n"
"v_mfma_f32_16x16x32_bf16 a[52:55], a[212:215], a[172:175], a[52:55]\n"
"v_mfma_f32_16x16x32_bf16 a[68:71], a[212:215], a[176:179], a[68:71]\n"
"buffer_load_dwordx4 v31, s[44:47], s61 offen offset:2048 lds\n"
"v_mfma_f32_16x16x32_bf16 a[84:87], a[212:215], a[180:183], a[84:87]\n"
"ds_read_b128 a[152:155], v17 offset:12288\n"
"v_mfma_f32_16x16x32_bf16 a[100:103], a[212:215], a[184:187], a[100:103]\n"
"v_mfma_f32_16x16x32_bf16 a[116:119], a[212:215], a[188:191], a[116:119]\n"
"buffer_load_dwordx4 v32, s[44:47], s61 offen offset:3072 lds\n"
"v_mfma_f32_16x16x32_bf16 a[8:11], a[216:219], a[160:163], a[8:11]\n"
"ds_read_b128 a[156:159], v17 offset:14336\n"
"v_mfma_f32_16x16x32_bf16 a[24:27], a[216:219], a[164:167], a[24:27]\n"
"s_waitcnt vmcnt(16)\n"
"v_mfma_f32_16x16x32_bf16 a[40:43], a[216:219], a[168:171], a[40:43]\n"
"ds_read_b64_tr_b16 a[192:193], v21 offset:8192\n"
"v_mfma_f32_16x16x32_bf16 a[56:59], a[216:219], a[172:175], a[56:59]\n"
"ds_read_b64_tr_b16 a[194:195], v25 offset:8192\n"
"s_add_u32 m0, s90, s71\n"
"v_mfma_f32_16x16x32_bf16 a[72:75], a[216:219], a[176:179], a[72:75]\n"
"s_cbranch_vccz .Lwn_xs2\n"
"buffer_load_dwordx4 v41, s[40:43], s58 offen lds\n"
".Lwn_xs2:\n"
"v_mfma_f32_16x16x32_bf16 a[88:91], a[216:219], a[180:183], a[88:91]\n"
"ds_read_b64_tr_b16 a[196:197], v22 offset:8192\n"
"v_mfma_f32_16x16x32_bf16 a[104:107], a[216:219], a[184:187], a[104:107]\n"
"ds_read_b64_tr_b16 a[198:199], v26 offset:8192\n"
"v_mfma_f32_16x16x32_bf16 a[120:123], a[216:219], a[188:191], a[120:123]\n"
"v_mfma_f32_16x16x32_bf16 a[12:15], a[220:223], a[160:163], a[12:15]\n"
"ds_read_b64_tr_b16 a[200:201], v23 offset:8192\n"
"v_mfma_f32_16x16x32_bf16 a[28:31], a[220:223], a[164:167], a[28:31]\n"
"ds_read_b64_tr_b16 a[202:203], v27 offset:8192\n"
"s_add_u32 m0, s91, s71\n"
"v_mfma_f32_16x16x32_bf16 a[44:47], a[220:223], a[168:171], a[44:47]\n"
"s_cbranch_vccz .Lwn_xs3\n"
"buffer_load_dwordx4 v41, s[40:43], s59 offen lds\n"
".Lwn_xs3:\n"
"v_mfma_f32_16x16x32_bf16 a[60:63], a[220:223], a[172:175], a[60:63]\n"
"ds_read_b64_tr_b16 a[204:205], v24 offset:8192\n"
"v_mfma_f32_16x16x32_bf16 a[76:79], a[220:223], a[176:179], a[76:79]\n"
"ds_read_b64_tr_b16 a[206:207], v28 offset:8192\n"
"v_mfma_f32_16x16x32_bf16 a[92:95], a[220:223], a[180:183], a[92:95]\n"
"v_mfma_f32_16x16x32_bf16 a[108:111], a[220:223], a[184:187], a[108:111]\n"
"s_add_u32 m0, s62, 8192\n"
"s_add_u32 s61, s61, 0x8000\n"
"v_mfma_f32_16x16x32_bf16 a[124:127], a[220:223], a[188:191], a[124:127]\n"
"s_waitcnt lgkmcnt(4)\n"
"v_mfma_f32_16x16x32_bf16 a[0:3], a[192:195], a[128:131], a[0:3]\n"
"ds_read_b128 a[160:163], v17 offset:512\n"
"v_mfma_f32_16x16x32_bf16 a[16:19], a[192:195], a[132:135], a[16:19]\n"
"ds_read_b128 a[164:167], v17 offset:2560\n"
"v_mfma_f32_16x16x32_bf16 a[32:35], a[192:195], a[136:139], a[32:35]\n"
"v_mfma_f32_16x16x32_bf16 a[48:51], a[192:195], a[140:143], a[48:51]\n"
"ds_read_b128 a[168:171], v17 offset:4608\n"
"v_mfma_f32_16x16x32_bf16 a[64:67], a[192:195], a[144:147], a[64:67]\n"
"ds_read_b128 a[172:175], v17 offset:6656\n"
"v_mfma_f32_16x16x32_bf16 a[80:83], a[192:195], a[148:151], a[80:83]\n"
"s_waitcnt lgkmcnt(4)\n"
"v_mfma_f32_16x16x32_bf16 a[96:99], a[192:195], a[152:155], a[96:99]\n"
"buffer_load_dwordx4 v29, s[44:47], s61 offen offset:0 lds\n"
"v_mfma_f32_16x16x32_bf16 a[112:115], a[192:195], a[156:159], a[112:115]\n"
"ds_read_b128 a[176:179], v17 offset:8704\n"
"v_mfma_f32_16x16x32_bf16 a[4:7], a[196:199], a[128:131], a[4:7]\n"
"v_mfma_f32_16x16x32_bf16 a[20:23], a[196:199], a[132:135], a[20:23]\n"
"buffer_load_dwordx4 v30, s[44:47], s61 offen offset:1024 lds\n"
"v_mfma_f32_16x16x32_bf16 a[36:39], a[196:199], a[136:139], a[36:39]\n"
"ds_read_b128 a[180:183], v17 offset:10752\n"
"v_mfma_f32_16x16x32_bf16 a[52:55], a[196:199], a[140:143], a[52:55]\n"
"v_mfma_f32_16x16x32_bf16 a[68:71], a[196:199], a[144:147], a[68:71]\n"
"buffer_load_dwordx4 v31, s[44:47], s61 offen offset:2048 lds\n"
"v_mfma_f32_16x16x32_bf16 a[84:87], a[196:199], a[148:151], a[84:87]\n"
"ds_read_b128 a[184:187], v17 offset:12800\n"
"v_mfma_f32_16x16x32_bf16 a[100:103], a[196:199], a[152:155], a[100:103]\n"
"v_mfma_f32_16x16x32_bf16 a[116:119], a[196:199], a[156:159], a[116:119]\n"
"buffer_load_dwordx4 v32, s[44:47], s61 offen offset:3072 lds\n"
"v_mfma_f32_16x16x32_bf16 a[8:11], a[200:203], a[128:131], a[8:11]\n"
"ds_read_b128 a[188:191], v17 offset:14848\n"
"v_mfma_f32_16x16x32_bf16 a[24:27], a[200:203], a[132:135], a[24:27]\n"
"s_waitcnt vmcnt(16)\n"
"v_mfma_f32_16x16x32_bf16 a[40:43], a[200:203], a[136:139], a[40:43]\n"
"ds_read_b64_tr_b16 a[208:209], v21 offset:12288\n"
"v_add_u32 v17, s63, v17\n"
"v_mfma_f32_16x16x32_bf16 a[56:59], a[200:203], a[140:143], a[56:59]\n"
"ds_read_b64_tr_b16 a[210:211], v25 offset:12288\n"
"s_add_u32 m0, s92, s71\n"
"v_mfma_f32_16x16x32_bf16 a[72:75], a[200:203], a[144:147], a[72:75]\n"
"s_cbranch_vccz .Lwn_xs4\n"
"buffer_load_dwordx4 v41, s[40:43], s60 offen lds\n"
".Lwn_xs4:\n"
"v_mfma_f32_16x16x32_bf16 a[88:91], a[200:203], a[148:151], a[88:91]\n"
"ds_read_b64_tr_b16 a[212:213], v22 offset:12288\n"
"v_mfma_f32_16x16x32_bf16 a[104:107], a[200:203], a[152:155], a[104:107]\n"
"ds_read_b64_tr_b16 a[214:215], v26 offset:12288\n"
"v_mfma_f32_16x16x32_bf16 a[120:123], a[200:203], a[156:159], a[120:123]\n"
"v_mfma_f32_16x16x32_bf16 a[12:15], a[204:207], a[128:131], a[12:15]\n"
"ds_read_b64_tr_b16 a[216:217], v23 offset:12288\n"
"v_mfma_f32_16x16x32_bf16 a[28:31], a[204:207], a[132:135], a[28:31]\n"
"ds_read_b64_tr_b16 a[218:219], v27 offset:12288\n"
"v_mfma_f32_16x16x32_bf16 a[44:47], a[204:207], a[136:139], a[44:47]\n"
"v_mfma_f32_16x16x32_bf16 a[60:63], a[204:207], a[140:143], a[60:63]\n"
"ds_read_b64_tr_b16 a[220:221], v24 offset:12288\n"
"v_mfma_f32_16x16x32_bf16 a[76:79], a[204:207], a[144:147], a[76:79]\n"
"ds_read_b64_tr_b16 a[222:223], v28 offset:12288\n"
"v_mfma_f32_16x16x32_bf16 a[92:95], a[204:207], a[148:151], a[92:95]\n"
"v_mfma_f32_16x16x32_bf16 a[108:111], a[204:207], a[152:155], a[108:111]\n"
"s_add_u32 m0, s62, 12288\n"
"s_add_u32 s61, s52, s53\n"
"v_mfma_f32_16x16x32_bf16 a[124:127], a[204:207], a[156:159], a[124:127]\n"
"s_waitcnt lgkmcnt(4)\n"
"v_mfma_f32_16x16x32_bf16 a[0:3], a[208:211], a[160:163], a[0:3]\n"
"ds_read_b128 a[128:131], v18 offset:0\n"
"v_mfma_f32_16x16x32_bf16 a[16:19], a[208:211], a[164:167], a[16:19]\n"
"ds_read_b128 a[132:135], v18 offset:2048\n"
"v_mfma_f32_16x16x32_bf16 a[32:35], a[208:211], a[168:171], a[32:35]\n"
"v_mfma_f32_16x16x32_bf16 a[48:51], a[208:211], a[172:175], a[48:51]\n"
"ds_read_b128 a[136:139], v18 offset:4096\n"
"v_mfma_f32_16x16x32_bf16 a[64:67], a[208:211], a[176:179], a[64:67]\n"
"ds_read_b128 a[140:143], v18 offset:6144\n"
"v_mfma_f32_16x16x32_bf16 a[80:83], a[208:211], a[180:183], a[80:83]\n"
"s_waitcnt lgkmcnt(4)\n"
"v_mfma_f32_16x16x32_bf16 a[96:99], a[208:211], a[184:187], a[96:99]\n"
"buffer_load_dwordx4 v29, s[44:47], s61 offen offset:0 lds\n"
"v_mfma_f32_16x16x32_bf16 a[112:115], a[208:211], a[188:191], a[112:115]\n"
"ds_read_b128 a[144:147], v18 offset:8192\n"
"v_mfma_f32_16x16x32_bf16 a[4:7], a[212:215], a[160:163], a[4:7]\n"
"v_mfma_f32_16x16x32_bf16 a[20:23], a[212:215], a[164:167], a[20:23]\n"
"buffer_load_dwordx4 v30, s[44:47], s61 offen offset:1024 lds\n"
"v_mfma_f32_16x16x32_bf16 a[36:39], a[212:215], a[168:171], a[36:39]\n"
"ds_read_b128 a[148:151], v18 offset:10240\n"
"v_mfma_f32_16x16x32_bf16 a[52:55], a[212:215], a[172:175], a[52:55]\n"
"v_mfma_f32_16x16x32_bf16 a[68:71], a[212:215], a[176:179], a[68:71]\n"
"buffer_load_dwordx4 v31, s[44:47], s61 offen offset:2048 lds\n"
"v_mfma_f32_16x16x32_bf16 a[84:87], a[212:215], a[180:183], a[84:87]\n"
"ds_read_b128 a[152:155], v18 offset:12288\n"
"v_mfma_f32_16x16x32_bf16 a[100:103], a[212:215], a[184:187], a[100:103]\n"
"v_mfma_f32_16x16x32_bf16 a[116:119], a[212:215], a[188:191], a[116:119]\n"
"buffer_load_dwordx4 v32, s[44:47], s61 offen offset:3072 lds\n"
"v_mfma_f32_16x16x32_bf16 a[8:11], a[216:219], a[160:163], a[8:11]\n"
"ds_read_b128 a[156:159], v18 offset:14336\n"
"v_mfma_f32_16x16x32_bf16 a[24:27], a[216:219], a[164:167], a[24:27]\n"
"s_waitcnt vmcnt(16)\n"
"v_mfma_f32_16x16x32_bf16 a[40:43], a[216:219], a[168:171], a[40:43]\n"
"ds_read_b64_tr_b16 a[192:193], v21 offset:16384\n"
"v_mfma_f32_16x16x32_bf16 a[56:59], a[216:219], a[172:175], a[56:59]\n"
"ds_read_b64_tr_b16 a[194:195], v25 offset:16384\n"
"v_mfma_f32_16x16x32_bf16 a[72:75], a[216:219], a[176:179], a[72:75]\n"
"s_xor_b32 s53, s54, s39\n"
"s_add_u32 s71, s71, 17408\n"
"v_mfma_f32_16x16x32_bf16 a[88:91], a[216:219], a[180:183], a[88:91]\n"
"ds_read_b64_tr_b16 a[196:197], v22 offset:16384\n"
"v_mfma_f32_16x16x32_bf16 a[104:107], a[216:219], a[184:187], a[104:107]\n"
"ds_read_b64_tr_b16 a[198:199], v26 offset:16384\n"
"v_mfma_f32_16x16x32_bf16 a[120:123], a[216:219], a[188:191], a[120:123]\n"
"v_mfma_f32_16x16x32_bf16 a[12:15], a[220:223], a[160:163], a[12:15]\n"
"ds_read_b64_tr_b16 a[200:201], v23 offset:16384\n"
"v_mfma_f32_16x16x32_bf16 a[28:31], a[220:223], a[164:167], a[28:31]\n"
"ds_read_b64_tr_b16 a[202:203], v27 offset:16384\n"
"v_mfma_f32_16x16x32_bf16 a[44:47], a[220:223], a[168:171], a[44:47]\n"
"v_mfma_f32_16x16x32_bf16 a[60:63], a[220:223], a[172:175], a[60:63]\n"
"ds_read_b64_tr_b16 a[204:205], v24 offset:16384\n"
"v_mfma_f32_16x16x32_bf16 a[76:79], a[220:223], a[176:179], a[76:79]\n"
"ds_read_b64_tr_b16 a[206:207], v28 offset:16384\n"
"v_mfma_f32_16x16x32_bf16 a[92:95], a[220:223], a[180:183], a[92:95]\n"
"v_mfma_f32_16x16x32_bf16 a[108:111], a[220:223], a[184:187], a[108:111]\n"
"s_add_u32 m0, s62, 16384\n"
"s_add_u32 s61, s61, 0x8000\n"
"v_mfma_f32_16x16x32_bf16 a[124:127], a[220:223], a[188:191], a[124:127]\n"
"s_waitcnt lgkmcnt(4)\n"
"v_mfma_f32_16x16x32_bf16 a[0:3], a[192:195], a[128:131], a[0:3]\n"
"ds_read_b128 a[160:163], v18 offset:512\n"
"v_mfma_f32_16x16x32_bf16 a[16:19], a[192:195], a[132:135], a[16:19]\n"
"ds_read_b128 a[164:167], v18 offset:2560\n"
"v_mfma_f32_16x16x32_bf16 a[32:35], a[192:195], a[136:139], a[32:35]\n"
"v_mfma_f32_16x16x32_bf16 a[48:51], a[192:195], a[140:143], a[48:51]\n"
"ds_read_b128 a[168:171], v18 offset:4608\n"
"v_mfma_f32_16x16x32_bf16 a[64:67], a[192:195], a[144:147], a[64:67]\n"
"ds_read_b128 a[172:175], v18 offset:6656\n"
"v_mfma_f32_16x16x32_bf16 a[80:83], a[192:195], a[148:151], a[80:83]\n"
"s_waitcnt lgkmcnt(4)\n"
"v_mfma_f32_16x16x32_bf16 a[96:99], a[192:195], a[152:155], a[96:99]\n"
"buffer_load_dwordx4 v29, s[44:47], s61 offen offset:0 lds\n"
"v_mfma_f32_16x16x32_bf16 a[112:115], a[192:195], a[156:159], a[112:115]\n"
"ds_read_b128 a[176:179], v18 offset:8704\n"
"v_mfma_f32_16x16x32_bf16 a[4:7], a[196:199], a[128:131], a[4:7]\n"
"s_min_u32 s99, s93, .Lwn_end-.Lwn_p2-1024\n"
"s_add_u32 s93, s93, 4096\n"
"v_mfma_f32_16x16x32_bf16 a[20:23], a[196:199], a[132:135], a[20:23]\n"
"buffer_load_dwordx4 v30, s[44:47], s61 offen offset:1024 lds\n"
"v_mfma_f32_16x16x32_bf16 a[36:39], a[196:199], a[136:139], a[36:39]\n"
"ds_read_b128 a[180:183], v18 offset:10752\n"
"v_mfma_f32_16x16x32_bf16 a[52:55], a[196:199], a[140:143], a[52:55]\n"
"v_add_u32 v43, s99, v42\n"
"s_and_b32 s93, s93, 0x7fff\n"
"v_mfma_f32_16x16x32_bf16 a[68:71], a[196:199], a[144:147], a[68:71]\n"
"buffer_load_dwordx4 v31, s[44:47], s61 offen offset:2048 lds\n"
"v_mfma_f32_16x16x32_bf16 a[84:87], a[196:199], a[148:151], a[84:87]\n"
"ds_read_b128 a[184:187], v18 offset:12800\n"
"v_mfma_f32_16x16x32_bf16 a[100:103], a[196:199], a[152:155], a[100:103]\n"
"v_mfma_f32_16x16x32_bf16 a[116:119], a[196:199], a[156:159], a[116:119]\n"
"buffer_load_dwordx4 v32, s[44:47], s61 offen offset:3072 lds\n"
"v_mfma_f32_16x16x32_bf16 a[8:11], a[200:203], a[128:131], a[8:11]\n"
"ds_read_b128 a[188:191], v18 offset:14848\n"
"v_mfma_f32_16x16x32_bf16 a[24:27], a[200:203], a[132:135], a[24:27]\n"
"s_waitcnt vmcnt(16)\n"
"v_mfma_f32_16x16x32_bf16 a[40:43], a[200:203], a[136:139], a[40:43]\n"
"ds_read_b64_tr_b16 a[208:209], v21 offset:0\n"
"v_add_u32 v18, s63, v18\n"
"v_mfma_f32_16x16x32_bf16 a[56:59], a[200:203], a[140:143], a[56:59]\n"
"ds_read_b64_tr_b16 a[210:211], v25 offset:0\n"
"v_mfma_f32_16x16x32_bf16 a[72:75], a[200:203], a[144:147], a[72:75]\n"
"global_load_dwordx4 v[48:51], v43, s[94:95]\n"
"v_mfma_f32_16x16x32_bf16 a[88:91], a[200:203], a[148:151], a[88:91]\n"
"ds_read_b64_tr_b16 a[212:213], v22 offset:0\n"
"v_mfma_f32_16x16x32_bf16 a[104:107], a[200:203], a[152:155], a[104:107]\n"
"ds_read_b64_tr_b16 a[214:215], v26 offset:0\n"
"v_mfma_f32_16x16x32_bf16 a[120:123], a[200:203], a[156:159], a[120:123]\n"
"v_mfma_f32_16x16x32_bf16 a[12:15], a[204:207], a[128:131], a[12:15]\n"
"ds_read_b64_tr_b16 a[216:217], v23 offset:0\n"
"v_mfma_f32_16x16x32_bf16 a[28:31], a[204:207], a[132:135], a[28:31]\n"
"ds_read_b64_tr_b16 a[218:219], v27 offset:0\n"
"v_mfma_f32_16x16x32_bf16 a[44:47], a[204:207], a[136:139], a[44:47]\n"
"v_mfma_f32_16x16x32_bf16 a[60:63], a[204:207], a[140:143], a[60:63]\n"
"ds_read_b64_tr_b16 a[220:221], v24 offset:0\n"
"v_mfma_f32_16x16x32_bf16 a[76:79], a[204:207], a[144:147], a[76:79]\n"
"ds_read_b64_tr_b16 a[222:223], v28 offset:0\n"
"v_mfma_f32_16x16x32_bf16 a[92:95], a[204:207], a[148:151], a[92:95]\n"
"v_mfma_f32_16x16x32_bf16 a[108:111], a[204:207], a[152:155], a[108:111]\n"
"s_add_u32 m0, s62, 0\n"
"s_add_u32 s61, s48, s53\n"
"v_mfma_f32_16x16x32_bf16 a[124:127], a[204:207], a[156:159], a[124:127]\n"
"s_waitcnt lgkmcnt(4)\n"
"v_mfma_f32_16x16x32_bf16 a[0:3], a[208:211], a[160:163], a[0:3]\n"
"ds_read_b128 a[128:131], v19 offset:0\n"
"v_mfma_f32_16x16x32_bf16 a[16:19], a[208:211], a[164:167], a[16:19]\n"
"ds_read_b128 a[132:135], v19 offset:2048\n"
"v_mfma_f32_16x16x32_bf16 a[32:35], a[208:211], a[168:171], a[32:35]\n"
"v_mfma_f32_16x16x32_bf16 a[48:51], a[208:211], a[172:175], a[48:51]\n"
"ds_read_b128 a[136:139], v19 offset:4096\n"
"v_mfma_f32_16x16x32_bf16 a[64:67], a[208:211], a[176:179], a[64:67]\n"
"ds_read_b128 a[140:143], v19 offset:6144\n"
"v_mfma_f32_16x16x32_bf16 a[80:83], a[208:211], a[180:183], a[80:83]\n"
"s_waitcnt lgkmcnt(4)\n"
"v_mfma_f32_16x16x32_bf16 a[96:99], a[208:211], a[184:187], a[96:99]\n"
"buffer_load_dwordx4 v29, s[44:47], s61 offen offset:0 lds\n"
"v_mfma_f32_16x16x32_bf16 a[112:115], a[208:211], a[188:191], a[112:115]\n"
"ds_read_b128 a[144:147], v19 offset:8192\n"
"v_mfma_f32_16x16x32_bf16 a[4:7], a[212:215], a[160:163], a[4:7]\n"
"v_mfma_f32_16x16x32_bf16 a[20:23], a[212:215], a[164:167], a[20:23]\n"
"buffer_load_dwordx4 v30, s[44:47], s61 offen offset:1024 lds\n"
"v_mfma_f32_16x16x32_bf16 a[36:39], a[212:215], a[168:171], a[36:39]\n"
"ds_read_b128 a[148:151], v19 offset:10240\n"
"v_mfma_f32_16x16x32_bf16 a[52:55], a[212:215], a[172:175], a[52:55]\n"
"v_mfma_f32_16x16x32_bf16 a[68:71], a[212:215], a[176:179], a[68:71]\n"
"buffer_load_dwordx4 v31, s[44:47], s61 offen offset:2048 lds\n"
"v_mfma_f32_16x16x32_bf16 a[84:87], a[212:215], a[180:183], a[84:87]\n"
"ds_read_b128 a[152:155], v19 offset:12288\n"
"v_mfma_f32_16x16x32_bf16 a[100:103], a[212:215], a[184:187], a[100:103]\n"
"v_mfma_f32_16x16x32_bf16 a[116:119], a[212:215], a[188:191], a[116:119]\n"
"buffer_load_dwordx4 v32, s[44:47], s61 offen offset:3072 lds\n"
"v_mfma_f32_16x16x32_bf16 a[8:11], a[216:219], a[160:163], a[8:11]\n"
"ds_read_b128 a[156:159], v19 offset:14336\n"
"v_mfma_f32_16x16x32_bf16 a[24:27], a[216:219], a[164:167], a[24:27]\n"
"s_waitcnt vmcnt(16)\n"
"v_mfma_f32_16x16x32_bf16 a[40:43], a[216:219], a[168:171], a[40:43]\n"
"ds_read_b64_tr_b16 a[192:193], v21 offset:4096\n"
"v_mfma_f32_16x16x32_bf16 a[56:59], a[216:219], a[172:175], a[56:59]\n"
"ds_read_b64_tr_b16 a[194:195], v25 offset:4096\n"
"v_mfma_f32_16x16x32_bf16 a[72:75], a[216:219], a[176:179], a[72:75]\n"
"v_mfma_f32_16x16x32_bf16 a[88:91], a[216:219], a[180:183], a[88:91]\n"
"ds_read_b64_tr_b16 a[196:197], v22 offset:4096\n"
"v_mfma_f32_16x16x32_bf16 a[104:107], a[216:219], a[184:187], a[104:107]\n"
"ds_read_b64_tr_b16 a[198:199], v26 offset:4096\n"
"v_mfma_f32_16x16x32_bf16 a[120:123], a[216:219], a[188:191], a[120:123]\n"
"v_mfma_f32_16x16x32_bf16 a[12:15], a[220:223], a[160:163], a[12:15]\n"
"ds_read_b64_tr_b16 a[200:201], v23 offset:4096\n"
"v_mfma_f32_16x16x32_bf16 a[28:31], a[220:223], a[164:167], a[28:31]\n"
"ds_read_b64_tr_b16 a[202:203], v27 offset:4096\n"
"v_mfma_f32_16x16x32_bf16 a[44:47], a[220:223], a[168:171], a[44:47]\n"
"v_mfma_f32_16x16x32_bf16 a[60:63], a[220:223], a[172:175], a[60:63]\n"
"ds_read_b64_tr_b16 a[204:205], v24 offset:4096\n"
"v_mfma_f32_16x16x32_bf16 a[76:79], a[220:223], a[176:179], a[76:79]\n"
"ds_read_b64_tr_b16 a[206:207], v28 offset:4096\n"
"v_mfma_f32_16x16x32_bf16 a[92:95], a[220:223], a[180:183], a[92:95]\n"
"v_mfma_f32_16x16x32_bf16 a[108:111], a[220:223], a[184:187], a[108:111]\n"
"s_add_u32 m0, s62, 4096\n"
"s_add_u32 s61, s61, 0x8000\n"
"v_mfma_f32_16x16x32_bf16 a[124:127], a[220:223], a[188:191], a[124:127]\n"
"s_waitcnt lgkmcnt(4)\n"
"v_mfma_f32_16x16x32_bf16 a[0:3], a[192:195], a[128:131], a[0:3]\n"
"ds_read_b128 a[160:163], v19 offset:512\n"
"v_mfma_f32_16x16x32_bf16 a[16:19], a[192:195], a[132:135], a[16:19]\n"
"ds_read_b128 a[164:167], v19 offset:2560\n"
"v_mfma_f32_16x16x32_bf16 a[32:35], a[192:195], a[136:139], a[32:35]\n"
"v_mfma_f32_16x16x32_bf16 a[48:51], a[192:195], a[140:143], a[48:51]\n"
"ds_read_b128 a[168:171], v19 offset:4608\n"
"v_mfma_f32_16x16x32_bf16 a[64:67], a[192:195], a[144:147], a[64:67]\n"
"ds_read_b128 a[172:175], v19 offset:6656\n"
"v_mfma_f32_16x16x32_bf16 a[80:83], a[192:195], a[148:151], a[80:83]\n"
"s_waitcnt lgkmcnt(4)\n"
"v_mfma_f32_16x16x32_bf16 a[96:99], a[192:195], a[152:155], a[96:99]\n"
"buffer_load_dwordx4 v29, s[44:47], s61 offen offset:0 lds\n"
"v_mfma_f32_16x16x32_bf16 a[112:115], a[192:195], a[156:159], a[112:115]\n"
"ds_read_b128 a[176:179], v19 offset:8704\n"
"v_mfma_f32_16x16x32_bf16 a[4:7], a[196:199], a[128:131], a[4:7]\n"
"s_min_u32 s99, s93, .Lwn_end-.Lwn_p2-1024\n"
"s_add_u32 s93, s93, 4096\n"
"v_mfma_f32_16x16x32_bf16 a[20:23], a[196:199], a[132:135], a[20:23]\n"
"buffer_load_dwordx4 v30, s[44:47], s61 offen offset:1024 lds\n"
"v_mfma_f32_16x16x32_bf16 a[36:39], a[196:199], a[136:139], a[36:39]\n"
"ds_read_b128 a[180:183], v19 offset:10752\n"
"v_mfma_f32_16x16x32_bf16 a[52:55], a[196:199], a[140:143], a[52:55]\n"
"v_add_u32 v43, s99, v42\n"
"s_and_b32 s93, s93, 0x7fff\n"
"v_mfma_f32_16x16x32_bf16 a[68:71], a[196:199], a[144:147], a[68:71]\n"
"buffer_load_dwordx4 v31, s[44:47], s61 offen offset:2048 lds\n"
"v_mfma_f32_16x16x32_bf16 a[84:87], a[196:199], a[148:151], a[84:87]\n"
"ds_read_b128 a[184:187], v19 offset:12800\n"
"v_mfma_f32_16x16x32_bf16 a[100:103], a[196:199], a[152:155], a[100:103]\n"
"v_mfma_f32_16x16x32_bf16 a[116:119], a[196:199], a[156:159], a[116:119]\n"
"buffer_load_dwordx4 v32, s[44:47], s61 offen offset:3072 lds\n"
"v_mfma_f32_16x16x32_bf16 a[8:11], a[200:203], a[128:131], a[8:11]\n"
"ds_read_b128 a[188:191], v19 offset:14848\n"
"v_mfma_f32_16x16x32_bf16 a[24:27], a[200:203], a[132:135], a[24:27]\n"
"s_waitcnt vmcnt(16)\n"
"v_mfma_f32_16x16x32_bf16 a[40:43], a[200:203], a[136:139], a[40:43]\n"
"ds_read_b64_tr_b16 a[208:209], v21 offset:8192\n"
"v_add_u32 v19, s63, v19\n"
"v_mfma_f32_16x16x32_bf16 a[56:59], a[200:203], a[140:143], a[56:59]\n"
"ds_read_b64_tr_b16 a[210:211], v25 offset:8192\n"
"v_mfma_f32_16x16x32_bf16 a[72:75], a[200:203], a[144:147], a[72:75]\n"
"global_load_dwordx4 v[48:51], v43, s[94:95]\n"
"v_mfma_f32_16x16x32_bf16 a[88:91], a[200:203], a[148:151], a[88:91]\n"
"ds_read_b64_tr_b16 a[212:213], v22 offset:8192\n"
"v_mfma_f32_16x16x32_bf16 a[104:107], a[200:203], a[152:155], a[104:107]\n"
"ds_read_b64_tr_b16 a[214:215], v26 offset:8192\n"
"v_mfma_f32_16x16x32_bf16 a[120:123], a[200:203], a[156:159], a[120:123]\n"
"v_mfma_f32_16x16x32_bf16 a[12:15], a[204:207], a[128:131], a[12:15]\n"
"ds_read_b64_tr_b16 a[216:217], v23 offset:8192\n"
"v_mfma_f32_16x16x32_bf16 a[28:31], a[204:207], a[132:135], a[28:31]\n"
"ds_read_b64_tr_b16 a[218:219], v27 offset:8192\n"
"v_mfma_f32_16x16x32_bf16 a[44:47], a[204:207], a[136:139], a[44:47]\n"
"v_mfma_f32_16x16x32_bf16 a[60:63], a[204:207], a[140:143], a[60:63]\n"
"ds_read_b64_tr_b16 a[220:221], v24 offset:8192\n"
"v_mfma_f32_16x16x32_bf16 a[76:79], a[204:207], a[144:147], a[76:79]\n"
"ds_read_b64_tr_b16 a[222:223], v28 offset:8192\n"
"v_mfma_f32_16x16x32_bf16 a[92:95], a[204:207], a[148:151], a[92:95]\n"
"v_mfma_f32_16x16x32_bf16 a[108:111], a[204:207], a[152:155], a[108:111]\n"
"s_add_u32 m0, s62, 8192\n"
"s_add_u32 s61, s49, s53\n"
"v_mfma_f32_16x16x32_bf16 a[124:127], a[204:207], a[156:159], a[124:127]\n"
"s_waitcnt lgkmcnt(4)\n"
"v_mfma_f32_16x16x32_bf16 a[0:3], a[208:211], a[160:163], a[0:3]\n"
"ds_read_b128 a[128:131], v20 offset:0\n"
"v_mfma_f32_16x16x32_bf16 a[16:19], a[208:211], a[164:167], a[16:19]\n"
"ds_read_b128 a[132:135], v20 offset:2048\n"
"v_mfma_f32_16x16x32_bf16 a[32:35], a[208:211], a[168:171], a[32:35]\n"
"v_mfma_f32_16x16x32_bf16 a[48:51], a[208:211], a[172:175], a[48:51]\n"
"ds_read_b128 a[136:139], v20 offset:4096\n"
"v_mfma_f32_16x16x32_bf16 a[64:67], a[208:211], a[176:179], a[64:67]\n"
"ds_read_b128 a[140:143], v20 offset:6144\n"
"v_mfma_f32_16x16x32_bf16 a[80:83], a[208:211], a[180:183], a[80:83]\n"
"s_waitcnt lgkmcnt(4)\n"
"v_mfma_f32_16x16x32_bf16 a[96:99], a[208:211], a[184:187], a[96:99]\n"
"buffer_load_dwordx4 v29, s[44:47], s61 offen offset:0 lds\n"
"v_mfma_f32_16x16x32_bf16 a[112:115], a[208:211], a[188:191], a[112:115]\n"
"ds_read_b128 a[144:147], v20 offset:8192\n"
"v_mfma_f32_16x16x32_bf16 a[4:7], a[212:215], a[160:163], a[4:7]\n"
"v_mfma_f32_16x16x32_bf16 a[20:23], a[212:215], a[164:167], a[20:23]\n"
"buffer_load_dwordx4 v30, s[44:47], s61 offen offset:1024 lds\n"
"v_mfma_f32_16x16x32_bf16 a[36:39], a[212:215], a[168:171], a[36:39]\n"
"ds_read_b128 a[148:151], v20 offset:10240\n"
"v_mfma_f32_16x16x32_bf16 a[52:55], a[212:215], a[172:175], a[52:55]\n"
"v_mfma_f32_16x16x32_bf16 a[68:71], a[212:215], a[176:179], a[68:71]\n"
"buffer_load_dwordx4 v31, s[44:47], s61 offen offset:2048 lds\n"
"v_mfma_f32_16x16x32_bf16 a[84:87], a[212:215], a[180:183], a[84:87]\n"
"ds_read_b128 a[152:155], v20 offset:12288\n"
"v_mfma_f32_16x16x32_bf16 a[100:103], a[212:215], a[184:187], a[100:103]\n"
"v_mfma_f32_16x16x32_bf16 a[116:119], a[212:215], a[188:191], a[116:119]\n"
"buffer_load_dwordx4 v32, s[44:47], s61 offen offset:3072 lds\n"
"v_mfma_f32_16x16x32_bf16 a[8:11], a[216:219], a[160:163], a[8:11]\n"
"ds_read_b128 a[156:159], v20 offset:14336\n"
"v_mfma_f32_16x16x32_bf16 a[24:27], a[216:219], a[164:167], a[24:27]\n"
"s_waitcnt vmcnt(16)\n"
"v_mfma_f32_16x16x32_bf16 a[40:43], a[216:219], a[168:171], a[40:43]\n"
"ds_read_b64_tr_b16 a[192:193], v21 offset:12288\n"
"v_mfma_f32_16x16x32_bf16 a[56:59], a[216:219], a[172:175], a[56:59]\n"
"ds_read_b64_tr_b16 a[194:195], v25 offset:12288\n"
"v_mfma_f32_16x16x32_bf16 a[72:75], a[216:219], a[176:179], a[72:75]\n"
"v_mfma_f32_16x16x32_bf16 a[88:91], a[216:219], a[180:183], a[88:91]\n"
"ds_read_b64_tr_b16 a[196:197], v22 offset:12288\n"
"v_mfma_f32_16x16x32_bf16 a[104:107], a[216:219], a[184:187], a[104:107]\n"
"ds_read_b64_tr_b16 a[198:199], v26 offset:12288\n"
"v_mfma_f32_16x16x32_bf16 a[120:123], a[216:219], a[188:191], a[120:123]\n"
"v_mfma_f32_16x16x32_bf16 a[12:15], a[220:223], a[160:163], a[12:15]\n"
"ds_read_b64_tr_b16 a[200:201], v23 offset:12288\n"
"v_mfma_f32_16x16x32_bf16 a[28:31], a[220:223], a[164:167], a[28:31]\n"
"ds_read_b64_tr_b16 a[202:203], v27 offset:12288\n"
"v_mfma_f32_16x16x32_bf16 a[44:47], a[220:223], a[168:171], a[44:47]\n"
"v_mfma_f32_16x16x32_bf16 a[60:63], a[220:223], a[172:175], a[60:63]\n"
"ds_read_b64_tr_b16 a[204:205], v24 offset:12288\n"
"v_mfma_f32_16x16x32_bf16 a[76:79], a[220:223], a[176:179], a[76:79]\n"
"ds_read_b64_tr_b16 a[206:207], v28 offset:12288\n"
"v_mfma_f32_16x16x32_bf16 a[92:95], a[220:223], a[180:183], a[92:95]\n"
"v_mfma_f32_16x16x32_bf16 a[108:111], a[220:223], a[184:187], a[108:111]\n"
"s_add_u32 m0, s62, 12288\n"
"s_add_u32 s61, s61, 0x8000\n"
"v_mfma_f32_16x16x32_bf16 a[124:127], a[220:223], a[188:191], a[124:127]\n"
"s_waitcnt lgkmcnt(4)\n"
"v_mfma_f32_16x16x32_bf16 a[0:3], a[192:195], a[128:131], a[0:3]\n"
"ds_read_b128 a[160:163], v20 offset:512\n"
"v_mfma_f32_16x16x32_bf16 a[16:19], a[192:195], a[132:135], a[16:19]\n"
"ds_read_b128 a[164:167], v20 offset:2560\n"
"v_mfma_f32_16x16x32_bf16 a[32:35], a[192:195], a[136:139], a[32:35]\n"
"v_mfma_f32_16x16x32_bf16 a[48:51], a[192:195], a[140:143], a[48:51]\n"
"ds_read_b128 a[168:171], v20 offset:4608\n"
"v_mfma_f32_16x16x32_bf16 a[64:67], a[192:195], a[144:147], a[64:67]\n"
"ds_read_b128 a[172:175], v20 offset:6656\n"
"v_mfma_f32_16x16x32_bf16 a[80:83], a[192:195], a[148:151], a[80:83]\n"
"s_waitcnt lgkmcnt(4)\n"
"v_mfma_f32_16x16x32_bf16 a[96:99], a[192:195], a[152:155], a[96:99]\n"
"buffer_load_dwordx4 v29, s[44:47], s61 offen offset:0 lds\n"
"v_mfma_f32_16x16x32_bf16 a[112:115], a[192:195], a[156:159], a[112:115]\n"
"ds_read_b128 a[176:179], v20 offset:8704\n"
"v_mfma_f32_16x16x32_bf16 a[4:7], a[196:199], a[128:131], a[4:7]\n"
"v_mfma_f32_16x16x32_bf16 a[20:23], a[196:199], a[132:135], a[20:23]\n"
"buffer_load_dwordx4 v30, s[44:47], s61 offen offset:1024 lds\n"
"v_mfma_f32_16x16x32_bf16 a[36:39], a[196:199], a[136:139], a[36:39]\n"
"ds_read_b128 a[180:183], v20 offset:10752\n"
"v_mfma_f32_16x16x32_bf16 a[52:55], a[196:199], a[140:143], a[52:55]\n"
"v_mfma_f32_16x16x32_bf16 a[68:71], a[196:199], a[144:147], a[68:71]\n"
"buffer_load_dwordx4 v31, s[44:47], s61 offen offset:2048 lds\n"
"v_mfma_f32_16x16x32_bf16 a[84:87], a[196:199], a[148:151], a[84:87]\n"
"ds_read_b128 a[184:187], v20 offset:12800\n"
"v_mfma_f32_16x16x32_bf16 a[100:103], a[196:199], a[152:155], a[100:103]\n"
"v_mfma_f32_16x16x32_bf16 a[116:119], a[196:199], a[156:159], a[116:119]\n"
"buffer_load_dwordx4 v32, s[44:47], s61 offen offset:3072 lds\n"
"v_mfma_f32_16x16x32_bf16 a[8:11], a[200:203], a[128:131], a[8:11]\n"
"ds_read_b128 a[188:191], v20 offset:14848\n"
"v_mfma_f32_16x16x32_bf16 a[24:27], a[200:203], a[132:135], a[24:27]\n"
"s_waitcnt vmcnt(16)\n"
"v_mfma_f32_16x16x32_bf16 a[40:43], a[200:203], a[136:139], a[40:43]\n"
"ds_read_b64_tr_b16 a[208:209], v21 offset:16384\n"
"v_add_u32 v20, s63, v20\n"
"v_mfma_f32_16x16x32_bf16 a[56:59], a[200:203], a[140:143], a[56:59]\n"
"ds_read_b64_tr_b16 a[210:211], v25 offset:16384\n"
"v_mfma_f32_16x16x32_bf16 a[72:75], a[200:203], a[144:147], a[72:75]\n"
"v_mfma_f32_16x16x32_bf16 a[88:91], a[200:203], a[148:151], a[88:91]\n"
"ds_read_b64_tr_b16 a[212:213], v22 offset:16384\n"
"v_mfma_f32_16x16x32_bf16 a[104:107], a[200:203], a[152:155], a[104:107]\n"
"ds_read_b64_tr_b16 a[214:215], v26 offset:16384\n"
"v_mfma_f32_16x16x32_bf16 a[120:123], a[200:203], a[156:159], a[120:123]\n"
"v_mfma_f32_16x16x32_bf16 a[12:15], a[204:207], a[128:131], a[12:15]\n"
"ds_read_b64_tr_b16 a[216:217], v23 offset:16384\n"
"v_mfma_f32_16x16x32_bf16 a[28:31], a[204:207], a[132:135], a[28:31]\n"
"ds_read_b64_tr_b16 a[218:219], v27 offset:16384\n"
"v_mfma_f32_16x16x32_bf16 a[44:47], a[204:207], a[136:139], a[44:47]\n"
"v_mfma_f32_16x16x32_bf16 a[60:63], a[204:207], a[140:143], a[60:63]\n"
"ds_read_b64_tr_b16 a[220:221], v24 offset:16384\n"
"v_mfma_f32_16x16x32_bf16 a[76:79], a[204:207], a[144:147], a[76:79]\n"
"ds_read_b64_tr_b16 a[222:223], v28 offset:16384\n"
"v_mfma_f32_16x16x32_bf16 a[92:95], a[204:207], a[148:151], a[92:95]\n"
"s_barrier\n"
"v_mfma_f32_16x16x32_bf16 a[108:111], a[204:207], a[152:155], a[108:111]\n"
"s_add_u32 m0, s62, 16384\n"
"s_add_u32 s61, s50, s53\n"
"v_mfma_f32_16x16x32_bf16 a[124:127], a[204:207], a[156:159], a[124:127]\n"
"s_waitcnt lgkmcnt(4)\n"
"v_mfma_f32_16x16x32_bf16 a[0:3], a[208:211], a[160:163], a[0:3]\n"
"ds_read_b128 a[128:131], v16 offset:0\n"
"v_mfma_f32_16x16x32_bf16 a[16:19], a[208:211], a[164:167], a[16:19]\n"
"ds_read_b128 a[132:135], v16 offset:2048\n"
"v_mfma_f32_16x16x32_bf16 a[32:35], a[208:211], a[168:171], a[32:35]\n"
"v_mfma_f32_16x16x32_bf16 a[48:51], a[208:211], a[172:175], a[48:51]\n"
"ds_read_b128 a[136:139], v16 offset:4096\n"
"v_mfma_f32_16x16x32_bf16 a[64:67], a[208:211], a[176:179], a[64:67]\n"
"ds_read_b128 a[140:143], v16 offset:6144\n"
"v_mfma_f32_16x16x32_bf16 a[80:83], a[208:211], a[180:183], a[80:83]\n"
"s_waitcnt lgkmcnt(4)\n"
"v_mfma_f32_16x16x32_bf16 a[96:99], a[208:211], a[184:187], a[96:99]\n"
"buffer_load_dwordx4 v29, s[44:47], s61 offen offset:0 lds\n"
"v_mfma_f32_16x16x32_bf16 a[112:115], a[208:211], a[188:191], a[112:115]\n"
"ds_read_b128 a[144:147], v16 offset:8192\n"
"v_mfma_f32_16x16x32_bf16 a[4:7], a[212:215], a[160:163], a[4:7]\n"
"v_mfma_f32_16x16x32_bf16 a[20:23], a[212:215], a[164:167], a[20:23]\n"
"buffer_load_dwordx4 v30, s[44:47], s61 offen offset:1024 lds\n"
"v_mfma_f32_16x16x32_bf16 a[36:39], a[212:215], a[168:171], a[36:39]\n"
"ds_read_b128 a[148:151], v16 offset:10240\n"
"v_mfma_f32_16x16x32_bf16 a[52:55], a[212:215], a[172:175], a[52:55]\n"
"v_mfma_f32_16x16x32_bf16 a[68:71], a[212:215], a[176:179], a[68:71]\n"
"buffer_load_dwordx4 v31, s[44:47], s61 offen offset:2048 lds\n"
"v_mfma_f32_16x16x32_bf16 a[84:87], a[212:215], a[180:183], a[84:87]\n"
"ds_read_b128 a[152:155], v16 offset:12288\n"
"v_mfma_f32_16x16x32_bf16 a[100:103], a[212:215], a[184:187], a[100:103]\n"
"v_mfma_f32_16x16x32_bf16 a[116:119], a[212:215], a[188:191], a[116:119]\n"
"buffer_load_dwordx4 v32, s[44:47], s61 offen offset:3072 lds\n"
"v_mfma_f32_16x16x32_bf16 a[8:11], a[216:219], a[160:163], a[8:11]\n"
"ds_read_b128 a[156:159], v16 offset:14336\n"
"v_mfma_f32_16x16x32_bf16 a[24:27], a[216:219], a[164:167], a[24:27]\n"
"s_waitcnt vmcnt(16)\n"
"v_mfma_f32_16x16x32_bf16 a[40:43], a[216:219], a[168:171], a[40:43]\n"
"ds_read_b64_tr_b16 a[192:193], v21 offset:0\n"
"v_mfma_f32_16x16x32_bf16 a[56:59], a[216:219], a[172:175], a[56:59]\n"
"ds_read_b64_tr_b16 a[194:195], v25 offset:0\n"
"v_mfma_f32_16x16x32_bf16 a[72:75], a[216:219], a[176:179], a[72:75]\n"
"v_mfma_f32_16x16x32_bf16 a[88:91], a[216:219], a[180:183], a[88:91]\n"
"ds_read_b64_tr_b16 a[196:197], v22 offset:0\n"
"v_mfma_f32_16x16x32_bf16 a[104:107], a[216:219], a[184:187], a[104:107]\n"
"ds_read_b64_tr_b16 a[198:199], v26 offset:0\n"
"v_mfma_f32_16x16x32_bf16 a[120:123], a[216:219], a[188:191], a[120:123]\n"
"v_mfma_f32_16x16x32_bf16 a[12:15], a[220:223], a[160:163], a[12:15]\n"
"ds_read_b64_tr_b16 a[200:201], v23 offset:0\n"
"v_mfma_f32_16x16x32_bf16 a[28:31], a[220:223], a[164:167], a[28:31]\n"
"ds_read_b64_tr_b16 a[202:203], v27 offset:0\n"
"v_mfma_f32_16x16x32_bf16 a[44:47], a[220:223], a[168:171], a[44:47]\n"
"v_mfma_f32_16x16x32_bf16 a[60:63], a[220:223], a[172:175], a[60:63]\n"
"ds_read_b64_tr_b16 a[204:205], v24 offset:0\n"
"v_mfma_f32_16x16x32_bf16 a[76:79], a[220:223], a[176:179], a[76:79]\n"
"ds_read_b64_tr_b16 a[206:207], v28 offset:0\n"
"v_mfma_f32_16x16x32_bf16 a[92:95], a[220:223], a[180:183], a[92:95]\n"
"s_add_u32 m0, s62, 0\n"
"s_add_u32 s61, s61, 0x8000\n"
"v_mfma_f32_16x16x32_bf16 a[108:111], a[220:223], a[184:187], a[108:111]\n"
"s_add_u32 s55, s55, 1\n"
"s_cmp_lt_u32 s55, 4\n"
"v_mfma_f32_16x16x32_bf16 a[124:127], a[220:223], a[188:191], a[124:127]\n"
"s_waitcnt lgkmcnt(4)\n"
"s_cbranch_scc1 .Lwn_loop\n"
".Lwn_p2:\n"
"v_mfma_f32_16x16x32_bf16 v[128:131], a[192:195], a[128:131], 0\n"
"ds_read_b128 a[160:163], v16 offset:512\n"
"v_mfma_f32_16x16x32_bf16 v[144:147], a[192:195], a[132:135], 0\n"
"ds_read_b128 a[164:167], v16 offset:2560\n"
"v_mfma_f32_16x16x32_bf16 v[160:163], a[192:195], a[136:139], 0\n"
"v_mfma_f32_16x16x32_bf16 v[176:179], a[192:195], a[140:143], 0\n"
"ds_read_b128 a[168:171], v16 offset:4608\n"
"v_mfma_f32_16x16x32_bf16 v[192:195], a[192:195], a[144:147], 0\n"
"ds_read_b128 a[172:175], v16 offset:6656\n"
"v_mfma_f32_16x16x32_bf16 v[208:211], a[192:195], a[148:151], 0\n"
"s_waitcnt lgkmcnt(4)\n"
"v_mfma_f32_16x16x32_bf16 v[224:227], a[192:195], a[152:155], 0\n"
"buffer_load_dwordx4 v29, s[44:47], s61 offen offset:0 lds\n"
"v_mfma_f32_16x16x32_bf16 v[240:243], a[192:195], a[156:159], 0\n"
"ds_read_b128 a[176:179], v16 offset:8704\n"
"v_mfma_f32_16x16x32_bf16 v[132:135], a[196:199], a[128:131], 0\n"
"v_mfma_f32_16x16x32_bf16 v[148:151], a[196:199], a[132:135], 0\n"
"buffer_load_dwordx4 v30, s[44:47], s61 offen offset:1024 lds\n"
"v_mfma_f32_16x16x32_bf16 v[164:167], a[196:199], a[136:139], 0\n"
"ds_read_b128 a[180:183], v16 offset:10752\n"
"v_mfma_f32_16x16x32_bf16 v[180:183], a[196:199], a[140:143], 0\n"
"s_mov_b32 s71, 1.0\n"
"v_mfma_f32_16x16x32_bf16 v[196:199], a[196:199], a[144:147], 0\n"
"buffer_load_dwordx4 v31, s[44:47], s61 offen offset:2048 lds\n"
"v_mfma_f32_16x16x32_bf16 v[212:215], a[196:199], a[148:151], 0\n"
"ds_read_b128 a[184:187], v16 offset:12800\n"
"v_mfma_f32_16x16x32_bf16 v[228:231], a[196:199], a[152:155], 0\n"
"v_mfma_f32_16x16x32_bf16 v[244:247], a[196:199], a[156:159], 0\n"
"buffer_load_dwordx4 v32, s[44:47], s61 offen offset:3072 lds\n"
"v_mfma_f32_16x16x32_bf16 v[136:139], a[200:203], a[128:131], 0\n"
"ds_read_b128 a[188:191], v16 offset:14848\n"
"v_mfma_f32_16x16x32_bf16 v[152:155], a[200:203], a[132:135], 0\n"
"s_waitcnt vmcnt(16)\n"
"v_mfma_f32_16x16x32_bf16 v[168:171], a[200:203], a[136:139], 0\n"
"ds_read_b64_tr_b16 a[208:209], v21 offset:4096\n"
"v_add_u32 v16, 17408, v16\n"
"v_mfma_f32_16x16x32_bf16 v[184:187], a[200:203], a[140:143], 0\n"
"ds_read_b64_tr_b16 a[210:211], v25 offset:4096\n"
"v_mfma_f32_16x16x32_bf16 v[200:203], a[200:203], a[144:147], 0\n"
"v_mfma_f32_16x16x32_bf16 v[216:219], a[200:203], a[148:151], 0\n"
"ds_read_b64_tr_b16 a[212:213], v22 offset:4096\n"
"v_mfma_f32_16x16x32_bf16 v[232:235], a[200:203], a[152:155], 0\n"
"ds_read_b64_tr_b16 a[214:215], v26 offset:4096\n"
"v_mfma_f32_16x16x32_bf16 v[248:251], a[200:203], a[156:159], 0\n"
"v_mfma_f32_16x16x32_bf16 v[140:143], a[204:207], a[128:131], 0\n"
"ds_read_b64_tr_b16 a[216:217], v23 offset:4096\n"
"v_mfma_f32_16x16x32_bf16 v[156:159], a[204:207], a[132:135], 0\n"
"ds_read_b64_tr_b16 a[218:219], v27 offset:4096\n"
"v_mfma_f32_16x16x32_bf16 v[172:175], a[204:207], a[136:139], 0\n"
"v_mfma_f32_16x16x32_bf16 v[188:191], a[204:207], a[140:143], 0\n"
"ds_read_b64_tr_b16 a[220:221], v24 offset:4096\n"
"v_mfma_f32_16x16x32_bf16 v[204:207], a[204:207], a[144:147], 0\n"
"ds_read_b64_tr_b16 a[222:223], v28 offset:4096\n"
"v_mfma_f32_16x16x32_bf16 v[220:223], a[204:207], a[148:151], 0\n"
"v_mfma_f32_16x16x32_bf16 v[236:239], a[204:207], a[152:155], 0\n"
"s_add_u32 m0, s62, 4096\n"
"s_add_u32 s61, s51, s53\n"
"v_mfma_f32_16x16x32_bf16 v[252:255], a[204:207], a[156:159], 0\n"
"s_waitcnt lgkmcnt(4)\n"
"v_mfma_f32_16x16x32_bf16 v[128:131], a[208:211], a[160:163], v[128:131]\n"
"ds_read_b128 a[128:131], v17 offset:0\n"
"v_accvgpr_read_b32 v48, a0\n"
"v_mfma_f32_16x16x32_bf16 v[144:147], a[208:211], a[164:167], v[144:147]\n"
"ds_read_b128 a[132:135], v17 offset:2048\n"
"v_accvgpr_read_b32 v52, a8\n"
"v_mfma_f32_16x16x32_bf16 v[160:163], a[208:211], a[168:171], v[160:163]\n"
"v_mul_f32 v48, s68, v48\n"
"v_mul_f32 v52, s69, v52\n"
"v_mfma_f32_16x16x32_bf16 v[176:179], a[208:211], a[172:175], v[176:179]\n"
"ds_read_b128 a[136:139], v17 offset:4096\n"
"v_accvgpr_read_b32 v49, a1\n"
"v_mfma_f32_16x16x32_bf16 v[192:195], a[208:211], a[176:179], v[192:195]\n"
"ds_read_b128 a[140:143], v17 offset:6144\n"
"v_accvgpr_read_b32 v53, a9\n"
"v_mfma_f32_16x16x32_bf16 v[208:211], a[208:211], a[180:183], v[208:211]\n"
"s_waitcnt lgkmcnt(4)\n"
"v_mul_f32 v49, s68, v49\n"
"v_mfma_f32_16x16x32_bf16 v[224:227], a[208:211], a[184:187], v[224:227]\n"
"buffer_load_dwordx4 v29, s[44:47], s61 offen offset:0 lds\n"
"v_mul_f32 v53, s69, v53\n"
"v_mfma_f32_16x16x32_bf16 v[240:243], a[208:211], a[188:191], v[240:243]\n"
"ds_read_b128 a[144:147], v17 offset:8192\n"
"v_accvgpr_read_b32 v50, a2\n"
"v_mfma_f32_16x16x32_bf16 v[132:135], a[212:215], a[160:163], v[132:135]\n"
"v_exp_f32 v56, -|v48|\n"
"v_mfma_f32_16x16x32_bf16 v[148:151], a[212:215], a[164:167], v[148:151]\n"
"buffer_load_dwordx4 v30, s[44:47], s61 offen offset:1024 lds\n"
"v_add_f32 v60, 1.0, v56\n"
"v_mfma_f32_16x16x32_bf16 v[164:167], a[212:215], a[168:171], v[164:167]\n"
"ds_read_b128 a[148:151], v17 offset:10240\n"
"v_accvgpr_read_b32 v54, a10\n"
"v_mfma_f32_16x16x32_bf16 v[180:183], a[212:215], a[172:175], v[180:183]\n"
"v_exp_f32 v52, -v52\n"
"v_mfma_f32_16x16x32_bf16 v[196:199], a[212:215], a[176:179], v[196:199]\n"
"buffer_load_dwordx4 v31, s[44:47], s61 offen offset:2048 lds\n"
"v_fmac_f32 v60, v60, v52\n"
"v_mfma_f32_16x16x32_bf16 v[212:215], a[212:215], a[180:183], v[212:215]\n"
"ds_read_b128 a[152:155], v17 offset:12288\n"
"v_mul_f32 v50, s68, v50\n"
"v_mfma_f32_16x16x32_bf16 v[228:231], a[212:215], a[184:187], v[228:231]\n"
"v_rcp_f32 v60, v60\n"
"v_mfma_f32_16x16x32_bf16 v[244:247], a[212:215], a[188:191], v[244:247]\n"
"buffer_load_dwordx4 v32, s[44:47], s61 offen offset:3072 lds\n"
"v_fma_f32 v56, -v56, v60, v60\n"
"v_mfma_f32_16x16x32_bf16 v[136:139], a[216:219], a[160:163], v[136:139]\n"
"ds_read_b128 a[156:159], v17 offset:14336\n"
"v_bfi_b32 v56, s70, v56, v48\n"
"v_mfma_f32_16x16x32_bf16 v[152:155], a[216:219], a[164:167], v[152:155]\n"
"s_waitcnt vmcnt(16)\n"
"v_mul_f32 v54, s69, v54\n"
"v_mfma_f32_16x16x32_bf16 v[168:171], a[216:219], a[168:171], v[168:171]\n"
"ds_read_b64_tr_b16 a[192:193], v21 offset:8192\n"
"v_accvgpr_read_b32 v51, a3\n"
"v_mfma_f32_16x16x32_bf16 v[184:187], a[216:219], a[172:175], v[184:187]\n"
"ds_read_b64_tr_b16 a[194:195], v25 offset:8192\n"
"v_accvgpr_read_b32 v55, a11\n"
"v_mfma_f32_16x16x32_bf16 v[200:203], a[216:219], a[176:179], v[200:203]\n"
"v_exp_f32 v57, -|v49|\n"
"v_mfma_f32_16x16x32_bf16 v[216:219], a[216:219], a[180:183], v[216:219]\n"
"ds_read_b64_tr_b16 a[196:197], v22 offset:8192\n"
"v_add_f32 v61, 1.0, v57\n"
"v_mfma_f32_16x16x32_bf16 v[232:235], a[216:219], a[184:187], v[232:235]\n"
"ds_read_b64_tr_b16 a[198:199], v26 offset:8192\n"
"v_mul_f32 v51, s68, v51\n"
"v_mfma_f32_16x16x32_bf16 v[248:251], a[216:219], a[188:191], v[248:251]\n"
"v_exp_f32 v53, -v53\n"
"v_mfma_f32_16x16x32_bf16 v[140:143], a[220:223], a[160:163], v[140:143]\n"
"ds_read_b64_tr_b16 a[200:201], v23 offset:8192\n"
"v_fmac_f32 v61, v61, v53\n"
"v_mfma_f32_16x16x32_bf16 v[156:159], a[220:223], a[164:167], v[156:159]\n"
"ds_read_b64_tr_b16 a[202:203], v27 offset:8192\n"
"v_mul_f32 v55, s69, v55\n"
"v_mfma_f32_16x16x32_bf16 v[172:175], a[220:223], a[168:171], v[172:175]\n"
"v_rcp_f32 v61, v61\n"
"v_mfma_f32_16x16x32_bf16 v[188:191], a[220:223], a[172:175], v[188:191]\n"
"ds_read_b64_tr_b16 a[204:205], v24 offset:8192\n"
"v_fma_f32 v57, -v57, v61, v61\n"
"v_mfma_f32_16x16x32_bf16 v[204:207], a[220:223], a[176:179], v[204:207]\n"
"ds_read_b64_tr_b16 a[206:207], v28 offset:8192\n"
"v_bfi_b32 v57, s70, v57, v49\n"
"v_mfma_f32_16x16x32_bf16 v[220:223], a[220:223], a[180:183], v[220:223]\n"
"v_exp_f32 v58, -|v50|\n"
"v_mfma_f32_16x16x32_bf16 v[236:239], a[220:223], a[184:187], v[236:239]\n"
"s_add_u32 m0, s62, 8192\n"
"s_add_u32 s61, s61, 0x8000\n"
"v_mfma_f32_16x16x32_bf16 v[252:255], a[220:223], a[188:191], v[252:255]\n"
"s_waitcnt lgkmcnt(4)\n"
"v_add_f32 v62, 1.0, v58\n"
"v_mfma_f32_16x16x32_bf16 v[128:131], a[192:195], a[128:131], v[128:131]\n"
"ds_read_b128 a[160:163], v17 offset:512\n"
"v_cvt_pk_bf16_f32 v96, v56, v57\n"
"v_mfma_f32_16x16x32_bf16 v[144:147], a[192:195], a[132:135], v[144:147]\n"
"ds_read_b128 a[164:167], v17 offset:2560\n"
"v_accvgpr_read_b32 v64, a4\n"
"v_mfma_f32_16x16x32_bf16 v[160:163], a[192:195], a[136:139], v[160:163]\n"
"v_exp_f32 v54, -v54\n"
"v_mfma_f32_16x16x32_bf16 v[176:179], a[192:195], a[140:143], v[176:179]\n"
"ds_read_b128 a[168:171], v17 offset:4608\n"
"v_fmac_f32 v62, v62, v54\n"
"v_mfma_f32_16x16x32_bf16 v[192:195], a[192:195], a[144:147], v[192:195]\n"
"ds_read_b128 a[172:175], v17 offset:6656\n"
"v_accvgpr_read_b32 v68, a12\n"
"v_mfma_f32_16x16x32_bf16 v[208:211], a[192:195], a[148:151], v[208:211]\n"
"s_waitcnt lgkmcnt(4)\n"
"v_mul_f32 v64, s68, v64\n"
"v_mfma_f32_16x16x32_bf16 v[224:227], a[192:195], a[152:155], v[224:227]\n"
"buffer_load_dwordx4 v29, s[44:47], s61 offen offset:0 lds\n"
"v_mul_f32 v68, s69, v68\n"
"v_mfma_f32_16x16x32_bf16 v[240:243], a[192:195], a[156:159], v[240:243]\n"
"ds_read_b128 a[176:179], v17 offset:8704\n"
"v_accvgpr_read_b32 v65, a5\n"
"v_mfma_f32_16x16x32_bf16 v[132:135], a[196:199], a[128:131], v[132:135]\n"
"v_rcp_f32 v62, v62\n"
"v_mfma_f32_16x16x32_bf16 v[148:151], a[196:199], a[132:135], v[148:151]\n"
"buffer_load_dwordx4 v30, s[44:47], s61 offen offset:1024 lds\n"
"v_fma_f32 v58, -v58, v62, v62\n"
"v_mfma_f32_16x16x32_bf16 v[164:167], a[196:199], a[136:139], v[164:167]\n"
"ds_read_b128 a[180:183], v17 offset:10752\n"
"v_bfi_b32 v58, s70, v58, v50\n"
"v_mfma_f32_16x16x32_bf16 v[180:183], a[196:199], a[140:143], v[180:183]\n"
"v_exp_f32 v59, -|v51|\n"
"v_mfma_f32_16x16x32_bf16 v[196:199], a[196:199], a[144:147], v[196:199]\n"
"buffer_load_dwordx4 v31, s[44:47], s61 offen offset:2048 lds\n"
"v_add_f32 v63, 1.0, v59\n"
"v_mfma_f32_16x16x32_bf16 v[212:215], a[196:199], a[148:151], v[212:215]\n"
"ds_read_b128 a[184:187], v17 offset:12800\n"
"v_accvgpr_read_b32 v69, a13\n"
"v_mfma_f32_16x16x32_bf16 v[228:231], a[196:199], a[152:155], v[228:231]\n"
"v_exp_f32 v55, -v55\n"
"v_mfma_f32_16x16x32_bf16 v[244:247], a[196:199], a[156:159], v[244:247]\n"
"buffer_load_dwordx4 v32, s[44:47], s61 offen offset:3072 lds\n"
"v_fmac_f32 v63, v63, v55\n"
"v_mfma_f32_16x16x32_bf16 v[136:139], a[200:203], a[128:131], v[136:139]\n"
"ds_read_b128 a[188:191], v17 offset:14848\n"
"v_mul_f32 v65, s68, v65\n"
"v_mfma_f32_16x16x32_bf16 v[152:155], a[200:203], a[132:135], v[152:155]\n"
"s_waitcnt vmcnt(16)\n"
"v_mul_f32 v69, s69, v69\n"
"v_mfma_f32_16x16x32_bf16 v[168:171], a[200:203], a[136:139], v[168:171]\n"
"ds_read_b64_tr_b16 a[208:209], v21 offset:12288\n"
"v_add_u32 v17, 17408, v17\n"
"v_mfma_f32_16x16x32_bf16 v[184:187], a[200:203], a[140:143], v[184:187]\n"
"ds_read_b64_tr_b16 a[210:211], v25 offset:12288\n"
"v_accvgpr_read_b32 v66, a6\n"
"v_mfma_f32_16x16x32_bf16 v[200:203], a[200:203], a[144:147], v[200:203]\n"
"v_rcp_f32 v63, v63\n"
"v_mfma_f32_16x16x32_bf16 v[216:219], a[200:203], a[148:151], v[216:219]\n"
"ds_read_b64_tr_b16 a[212:213], v22 offset:12288\n"
"v_fma_f32 v59, -v59, v63, v63\n"
"v_mfma_f32_16x16x32_bf16 v[232:235], a[200:203], a[152:155], v[232:235]\n"
"ds_read_b64_tr_b16 a[214:215], v26 offset:12288\n"
"v_bfi_b32 v59, s70, v59, v51\n"
"v_mfma_f32_16x16x32_bf16 v[248:251], a[200:203], a[156:159], v[248:251]\n"
"v_exp_f32 v72, -|v64|\n"
"v_mfma_f32_16x16x32_bf16 v[140:143], a[204:207], a[128:131], v[140:143]\n"
"ds_read_b64_tr_b16 a[216:217], v23 offset:12288\n"
"v_cvt_pk_bf16_f32 v97, v58, v59\n"
"v_mfma_f32_16x16x32_bf16 v[156:159], a[204:207], a[132:135], v[156:159]\n"
"ds_read_b64_tr_b16 a[218:219], v27 offset:12288\n"
"v_add_f32 v76, 1.0, v72\n"
"v_mfma_f32_16x16x32_bf16 v[172:175], a[204:207], a[136:139], v[172:175]\n"
"v_exp_f32 v68, -v68\n"
"v_mfma_f32_16x16x32_bf16 v[188:191], a[204:207], a[140:143], v[188:191]\n"
"ds_read_b64_tr_b16 a[220:221], v24 offset:12288\n"
"v_fmac_f32 v76, v76, v68\n"
"v_mfma_f32_16x16x32_bf16 v[204:207], a[204:207], a[144:147], v[204:207]\n"
"ds_read_b64_tr_b16 a[222:223], v28 offset:12288\n"
"v_accvgpr_read_b32 v70, a14\n"
"v_mfma_f32_16x16x32_bf16 v[220:223], a[204:207], a[148:151], v[220:223]\n"
"v_rcp_f32 v76, v76\n"
"v_mfma_f32_16x16x32_bf16 v[236:239], a[204:207], a[152:155], v[236:239]\n"
"s_add_u32 m0, s62, 12288\n"
"s_add_u32 s61, s52, s53\n"
"v_mfma_f32_16x16x32_bf16 v[252:255], a[204:207], a[156:159], v[252:255]\n"
"s_waitcnt lgkmcnt(4)\n"
"v_fma_f32 v72, -v72, v76, v76\n"
"v_mfma_f32_16x16x32_bf16 v[128:131], a[208:211], a[160:163], v[128:131]\n"
"ds_read_b128 a[128:131], v18 offset:0\n"
"v_bfi_b32 v72, s70, v72, v64\n"
"v_mfma_f32_16x16x32_bf16 v[144:147], a[208:211], a[164:167], v[144:147]\n"
"ds_read_b128 a[132:135], v18 offset:2048\n"
"v_mul_f32 v66, s68, v66\n"
"v_mfma_f32_16x16x32_bf16 v[160:163], a[208:211], a[168:171], v[160:163]\n"
"v_exp_f32 v73, -|v65|\n"
"v_mfma_f32_16x16x32_bf16 v[176:179], a[208:211], a[172:175], v[176:179]\n"
"ds_read_b128 a[136:139], v18 offset:4096\n"
"v_add_f32 v77, 1.0, v73\n"
"v_mfma_f32_16x16x32_bf16 v[192:195], a[208:211], a[176:179], v[192:195]\n"
"ds_read_b128 a[140:143], v18 offset:6144\n"
"v_mul_f32 v70, s69, v70\n"
"v_mfma_f32_16x16x32_bf16 v[208:211], a[208:211], a[180:183], v[208:211]\n"
"s_waitcnt lgkmcnt(4)\n"
"v_accvgpr_read_b32 v67, a7\n"
"v_mfma_f32_16x16x32_bf16 v[224:227], a[208:211], a[184:187], v[224:227]\n"
"buffer_load_dwordx4 v29, s[44:47], s61 offen offset:0 lds\n"
"v_accvgpr_read_b32 v71, a15\n"
"v_mfma_f32_16x16x32_bf16 v[240:243], a[208:211], a[188:191], v[240:243]\n"
"ds_read_b128 a[144:147], v18 offset:8192\n"
"v_mul_f32 v67, s68, v67\n"
"v_mfma_f32_16x16x32_bf16 v[132:135], a[212:215], a[160:163], v[132:135]\n"
"v_exp_f32 v69, -v69\n"
"v_mfma_f32_16x16x32_bf16 v[148:151], a[212:215], a[164:167], v[148:151]\n"
"buffer_load_dwordx4 v30, s[44:47], s61 offen offset:1024 lds\n"
"v_fmac_f32 v77, v77, v69\n"
"v_mfma_f32_16x16x32_bf16 v[164:167], a[212:215], a[168:171], v[164:167]\n"
"ds_read_b128 a[148:151], v18 offset:10240\n"
"v_mul_f32 v71, s69, v71\n"
"v_mfma_f32_16x16x32_bf16 v[180:183], a[212:215], a[172:175], v[180:183]\n"
"v_rcp_f32 v77, v77\n"
"v_mfma_f32_16x16x32_bf16 v[196:199], a[212:215], a[176:179], v[196:199]\n"
"buffer_load_dwordx4 v31, s[44:47], s61 offen offset:2048 lds\n"
"v_fma_f32 v73, -v73, v77, v77\n"
"v_mfma_f32_16x16x32_bf16 v[212:215], a[212:215], a[180:183], v[212:215]\n"
"ds_read_b128 a[152:155], v18 offset:12288\n"
"v_bfi_b32 v73, s70, v73, v65\n"
"v_mfma_f32_16x16x32_bf16 v[228:231], a[212:215], a[184:187], v[228:231]\n"
"v_exp_f32 v74, -|v66|\n"
"v_mfma_f32_16x16x32_bf16 v[244:247], a[212:215], a[188:191], v[244:247]\n"
"buffer_load_dwordx4 v32, s[44:47], s61 offen offset:3072 lds\n"
"v_add_f32 v78, 1.0, v74\n"
"v_mfma_f32_16x16x32_bf16 v[136:139], a[216:219], a[160:163], v[136:139]\n"
"ds_read_b128 a[156:159], v18 offset:14336\n"
"v_cvt_pk_bf16_f32 v98, v72, v73\n"
"v_mfma_f32_16x16x32_bf16 v[152:155], a[216:219], a[164:167], v[152:155]\n"
"s_waitcnt vmcnt(16)\n"
"v_accvgpr_read_b32 v80, a16\n"
"v_mfma_f32_16x16x32_bf16 v[168:171], a[216:219], a[168:171], v[168:171]\n"
"ds_read_b64_tr_b16 a[192:193], v21 offset:16384\n"
"v_accvgpr_read_b32 v84, a24\n"
"v_mfma_f32_16x16x32_bf16 v[184:187], a[216:219], a[172:175], v[184:187]\n"
"ds_read_b64_tr_b16 a[194:195], v25 offset:16384\n"
"v_mul_f32 v80, s68, v80\n"
"v_mfma_f32_16x16x32_bf16 v[200:203], a[216:219], a[176:179], v[200:203]\n"
"s_add_u32 s53, s53, 0x10000\n"
"s_and_b32 s53, s53, 0x3ffff\n"
"v_mfma_f32_16x16x32_bf16 v[216:219], a[216:219], a[180:183], v[216:219]\n"
"ds_read_b64_tr_b16 a[196:197], v22 offset:16384\n"
"v_mul_f32 v84, s69, v84\n"
"v_mfma_f32_16x16x32_bf16 v[232:235], a[216:219], a[184:187], v[232:235]\n"
"ds_read_b64_tr_b16 a[198:199], v26 offset:16384\n"
"v_accvgpr_read_b32 v81, a17\n"
"v_mfma_f32_16x16x32_bf16 v[248:251], a[216:219], a[188:191], v[248:251]\n"
"v_exp_f32 v70, -v70\n"
"v_mfma_f32_16x16x32_bf16 v[140:143], a[220:223], a[160:163], v[140:143]\n"
"ds_read_b64_tr_b16 a[200:201], v23 offset:16384\n"
"v_fmac_f32 v78, v78, v70\n"
"v_mfma_f32_16x16x32_bf16 v[156:159], a[220:223], a[164:167], v[156:159]\n"
"ds_read_b64_tr_b16 a[202:203], v27 offset:16384\n"
"v_accvgpr_read_b32 v85, a25\n"
"v_mfma_f32_16x16x32_bf16 v[172:175], a[220:223], a[168:171], v[172:175]\n"
"v_rcp_f32 v78, v78\n"
"v_mfma_f32_16x16x32_bf16 v[188:191], a[220:223], a[172:175], v[188:191]\n"
"ds_read_b64_tr_b16 a[204:205], v24 offset:16384\n"
"v_fma_f32 v74, -v74, v78, v78\n"
"v_mfma_f32_16x16x32_bf16 v[204:207], a[220:223], a[176:179], v[204:207]\n"
"ds_read_b64_tr_b16 a[206:207], v28 offset:16384\n"
"v_bfi_b32 v74, s70, v74, v66\n"
"v_mfma_f32_16x16x32_bf16 v[220:223], a[220:223], a[180:183], v[220:223]\n"
"v_exp_f32 v75, -|v67|\n"
"v_mfma_f32_16x16x32_bf16 v[236:239], a[220:223], a[184:187], v[236:239]\n"
"s_add_u32 m0, s62, 16384\n"
"s_add_u32 s61, s61, 0x8000\n"
"v_mfma_f32_16x16x32_bf16 v[252:255], a[220:223], a[188:191], v[252:255]\n"
"s_waitcnt lgkmcnt(4)\n"
"v_add_f32 v79, 1.0, v75\n"
"v_mfma_f32_16x16x32_bf16 v[128:131], a[192:195], a[128:131], v[128:131]\n"
"ds_read_b128 a[160:163], v18 offset:512\n"
"v_mul_f32 v81, s68, v81\n"
"v_mfma_f32_16x16x32_bf16 v[144:147], a[192:195], a[132:135], v[144:147]\n"
"ds_read_b128 a[164:167], v18 offset:2560\n"
"v_mul_f32 v85, s69, v85\n"
"v_mfma_f32_16x16x32_bf16 v[160:163], a[192:195], a[136:139], v[160:163]\n"
"v_exp_f32 v71, -v71\n"
"v_mfma_f32_16x16x32_bf16 v[176:179], a[192:195], a[140:143], v[176:179]\n"
"ds_read_b128 a[168:171], v18 offset:4608\n"
"v_fmac_f32 v79, v79, v71\n"
"v_mfma_f32_16x16x32_bf16 v[192:195], a[192:195], a[144:147], v[192:195]\n"
"ds_read_b128 a[172:175], v18 offset:6656\n"
"v_accvgpr_read_b32 v82, a18\n"
"v_mfma_f32_16x16x32_bf16 v[208:211], a[192:195], a[148:151], v[208:211]\n"
"s_waitcnt lgkmcnt(4)\n"
"v_accvgpr_read_b32 v86, a26\n"
"v_mfma_f32_16x16x32_bf16 v[224:227], a[192:195], a[152:155], v[224:227]\n"
"buffer_load_dwordx4 v29, s[44:47], s61 offen offset:0 lds\n"
"v_mul_f32 v82, s68, v82\n"
"v_mfma_f32_16x16x32_bf16 v[240:243], a[192:195], a[156:159], v[240:243]\n"
"ds_read_b128 a[176:179], v18 offset:8704\n"
"v_mul_f32 v86, s69, v86\n"
"v_mfma_f32_16x16x32_bf16 v[132:135], a[196:199], a[128:131], v[132:135]\n"
"v_rcp_f32 v79, v79\n"
"v_mfma_f32_16x16x32_bf16 v[148:151], a[196:199], a[132:135], v[148:151]\n"
"buffer_load_dwordx4 v30, s[44:47], s61 offen offset:1024 lds\n"
"v_fma_f32 v75, -v75, v79, v79\n"
"v_mfma_f32_16x16x32_bf16 v[164:167], a[196:199], a[136:139], v[164:167]\n"
"ds_read_b128 a[180:183], v18 offset:10752\n"
"v_bfi_b32 v75, s70, v75, v67\n"
"v_mfma_f32_16x16x32_bf16 v[180:183], a[196:199], a[140:143], v[180:183]\n"
"v_exp_f32 v88, -|v80|\n"
"v_mfma_f32_16x16x32_bf16 v[196:199], a[196:199], a[144:147], v[196:199]\n"
"buffer_load_dwordx4 v31, s[44:47], s61 offen offset:2048 lds\n"
"v_cvt_pk_bf16_f32 v99, v74, v75\n"
"v_mfma_f32_16x16x32_bf16 v[212:215], a[196:199], a[148:151], v[212:215]\n"
"ds_read_b128 a[184:187], v18 offset:12800\n"
"v_add_f32 v92, 1.0, v88\n"
"v_mfma_f32_16x16x32_bf16 v[228:231], a[196:199], a[152:155], v[228:231]\n"
"v_exp_f32 v84, -v84\n"
"v_mfma_f32_16x16x32_bf16 v[244:247], a[196:199], a[156:159], v[244:247]\n"
"buffer_load_dwordx4 v32, s[44:47], s61 offen offset:3072 lds\n"
"v_fmac_f32 v92, v92, v84\n"
"v_mfma_f32_16x16x32_bf16 v[136:139], a[200:203], a[128:131], v[136:139]\n"
"ds_read_b128 a[188:191], v18 offset:14848\n"
"v_accvgpr_read_b32 v83, a19\n"
"v_mfma_f32_16x16x32_bf16 v[152:155], a[200:203], a[132:135], v[152:155]\n"
"s_waitcnt vmcnt(16)\n"
"v_accvgpr_read_b32 v87, a27\n"
"v_mfma_f32_16x16x32_bf16 v[168:171], a[200:203], a[136:139], v[168:171]\n"
"ds_read_b64_tr_b16 a[208:209], v21 offset:0\n"
"v_add_u32 v18, 17408, v18\n"
"v_mfma_f32_16x16x32_bf16 v[184:187], a[200:203], a[140:143], v[184:187]\n"
"ds_read_b64_tr_b16 a[210:211], v25 offset:0\n"
"v_mul_f32 v83, s68, v83\n"
"v_mfma_f32_16x16x32_bf16 v[200:203], a[200:203], a[144:147], v[200:203]\n"
"v_rcp_f32 v92, v92\n"
"v_mfma_f32_16x16x32_bf16 v[216:219], a[200:203], a[148:151], v[216:219]\n"
"ds_read_b64_tr_b16 a[212:213], v22 offset:0\n"
"v_fma_f32 v88, -v88, v92, v92\n"
"v_mfma_f32_16x16x32_bf16 v[232:235], a[200:203], a[152:155], v[232:235]\n"
"ds_read_b64_tr_b16 a[214:215], v26 offset:0\n"
"v_bfi_b32 v88, s70, v88, v80\n"
"v_mfma_f32_16x16x32_bf16 v[248:251], a[200:203], a[156:159], v[248:251]\n"
"v_exp_f32 v89, -|v81|\n"
"v_mfma_f32_16x16x32_bf16 v[140:143], a[204:207], a[128:131], v[140:143]\n"
"ds_read_b64_tr_b16 a[216:217], v23 offset:0\n"
"v_add_f32 v93, 1.0, v89\n"
"v_mfma_f32_16x16x32_bf16 v[156:159], a[204:207], a[132:135], v[156:159]\n"
"ds_read_b64_tr_b16 a[218:219], v27 offset:0\n"
"v_mul_f32 v87, s69, v87\n"
"v_mfma_f32_16x16x32_bf16 v[172:175], a[204:207], a[136:139], v[172:175]\n"
"v_exp_f32 v85, -v85\n"
"v_mfma_f32_16x16x32_bf16 v[188:191], a[204:207], a[140:143], v[188:191]\n"
"ds_read_b64_tr_b16 a[220:221], v24 offset:0\n"
"v_fmac_f32 v93, v93, v85\n"
"v_mfma_f32_16x16x32_bf16 v[204:207], a[204:207], a[144:147], v[204:207]\n"
"ds_read_b64_tr_b16 a[222:223], v28 offset:0\n"
"v_accvgpr_read_b32 v48, a20\n"
"v_mfma_f32_16x16x32_bf16 v[220:223], a[204:207], a[148:151], v[220:223]\n"
"v_rcp_f32 v93, v93\n"
"v_mfma_f32_16x16x32_bf16 v[236:239], a[204:207], a[152:155], v[236:239]\n"
"s_add_u32 m0, s62, 0\n"
"s_add_u32 s61, s48, s53\n"
"v_mfma_f32_16x16x32_bf16 v[252:255], a[204:207], a[156:159], v[252:255]\n"
"s_waitcnt lgkmcnt(4)\n"
"v_fma_f32 v89, -v89, v93, v93\n"
"v_mfma_f32_16x16x32_bf16 v[128:131], a[208:211], a[160:163], v[128:131]\n"
"ds_read_b128 a[128:131], v19 offset:0\n"
"v_bfi_b32 v89, s70, v89, v81\n"
"v_mfma_f32_16x16x32_bf16 v[144:147], a[208:211], a[164:167], v[144:147]\n"
"ds_read_b128 a[132:135], v19 offset:2048\n"
"v_cvt_pk_bf16_f32 v100, v88, v89\n"
"v_mfma_f32_16x16x32_bf16 v[160:163], a[208:211], a[168:171], v[160:163]\n"
"v_exp_f32 v90, -|v82|\n"
"v_mfma_f32_16x16x32_bf16 v[176:179], a[208:211], a[172:175], v[176:179]\n"
"ds_read_b128 a[136:139], v19 offset:4096\n"
"v_add_f32 v94, 1.0, v90\n"
"v_mfma_f32_16x16x32_bf16 v[192:195], a[208:211], a[176:179], v[192:195]\n"
"ds_read_b128 a[140:143], v19 offset:6144\n"
"v_accvgpr_read_b32 v52, a28\n"
"v_mfma_f32_16x16x32_bf16 v[208:211], a[208:211], a[180:183], v[208:211]\n"
"s_waitcnt lgkmcnt(4)\n"
"v_mul_f32 v48, s68, v48\n"
"v_mfma_f32_16x16x32_bf16 v[224:227], a[208:211], a[184:187], v[224:227]\n"
"buffer_load_dwordx4 v29, s[44:47], s61 offen offset:0 lds\n"
"v_mul_f32 v52, s69, v52\n"
"v_mfma_f32_16x16x32_bf16 v[240:243], a[208:211], a[188:191], v[240:243]\n"
"ds_read_b128 a[144:147], v19 offset:8192\n"
"v_accvgpr_read_b32 v49, a21\n"
"v_mfma_f32_16x16x32_bf16 v[132:135], a[212:215], a[160:163], v[132:135]\n"
"v_exp_f32 v86, -v86\n"
"v_mfma_f32_16x16x32_bf16 v[148:151], a[212:215], a[164:167], v[148:151]\n"
"buffer_load_dwordx4 v30, s[44:47], s61 offen offset:1024 lds\n"
"v_fmac_f32 v94, v94, v86\n"
"v_mfma_f32_16x16x32_bf16 v[164:167], a[212:215], a[168:171], v[164:167]\n"
"ds_read_b128 a[148:151], v19 offset:10240\n"
"v_accvgpr_read_b32 v53, a29\n"
"v_mfma_f32_16x16x32_bf16 v[180:183], a[212:215], a[172:175], v[180:183]\n"
"v_rcp_f32 v94, v94\n"
"v_mfma_f32_16x16x32_bf16 v[196:199], a[212:215], a[176:179], v[196:199]\n"
"buffer_load_dwordx4 v31, s[44:47], s61 offen offset:2048 lds\n"
"v_fma_f32 v90, -v90, v94, v94\n"
"v_mfma_f32_16x16x32_bf16 v[212:215], a[212:215], a[180:183], v[212:215]\n"
"ds_read_b128 a[152:155], v19 offset:12288\n"
"v_bfi_b32 v90, s70, v90, v82\n"
"v_mfma_f32_16x16x32_bf16 v[228:231], a[212:215], a[184:187], v[228:231]\n"
"v_exp_f32 v91, -|v83|\n"
"v_mfma_f32_16x16x32_bf16 v[244:247], a[212:215], a[188:191], v[244:247]\n"
"buffer_load_dwordx4 v32, s[44:47], s61 offen offset:3072 lds\n"
"v_add_f32 v95, 1.0, v91\n"
"v_mfma_f32_16x16x32_bf16 v[136:139], a[216:219], a[160:163], v[136:139]\n"
"ds_read_b128 a[156:159], v19 offset:14336\n"
"v_mul_f32 v49, s68, v49\n"
"v_mfma_f32_16x16x32_bf16 v[152:155], a[216:219], a[164:167], v[152:155]\n"
"s_waitcnt vmcnt(16)\n"
"v_mul_f32 v53, s69, v53\n"
"v_mfma_f32_16x16x32_bf16 v[168:171], a[216:219], a[168:171], v[168:171]\n"
"ds_read_b64_tr_b16 a[192:193], v21 offset:4096\n"
"v_accvgpr_read_b32 v50, a22\n"
"v_mfma_f32_16x16x32_bf16 v[184:187], a[216:219], a[172:175], v[184:187]\n"
"ds_read_b64_tr_b16 a[194:195], v25 offset:4096\n"
"v_accvgpr_read_b32 v54, a30\n"
"v_mfma_f32_16x16x32_bf16 v[200:203], a[216:219], a[176:179], v[200:203]\n"
"v_exp_f32 v87, -v87\n"
"v_mfma_f32_16x16x32_bf16 v[216:219], a[216:219], a[180:183], v[216:219]\n"
"ds_read_b64_tr_b16 a[196:197], v22 offset:4096\n"
"v_fmac_f32 v95, v95, v87\n"
"v_mfma_f32_16x16x32_bf16 v[232:235], a[216:219], a[184:187], v[232:235]\n"
"ds_read_b64_tr_b16 a[198:199], v26 offset:4096\n"
"v_mul_f32 v50, s68, v50\n"
"v_mfma_f32_16x16x32_bf16 v[248:251], a[216:219], a[188:191], v[248:251]\n"
"v_rcp_f32 v95, v95\n"
"v_mfma_f32_16x16x32_bf16 v[140:143], a[220:223], a[160:163], v[140:143]\n"
"ds_read_b64_tr_b16 a[200:201], v23 offset:4096\n"
"v_fma_f32 v91, -v91, v95, v95\n"
"v_mfma_f32_16x16x32_bf16 v[156:159], a[220:223], a[164:167], v[156:159]\n"
"ds_read_b64_tr_b16 a[202:203], v27 offset:4096\n"
"v_bfi_b32 v91, s70, v91, v83\n"
"v_mfma_f32_16x16x32_bf16 v[172:175], a[220:223], a[168:171], v[172:175]\n"
"v_exp_f32 v56, -|v48|\n"
"v_mfma_f32_16x16x32_bf16 v[188:191], a[220:223], a[172:175], v[188:191]\n"
"ds_read_b64_tr_b16 a[204:205], v24 offset:4096\n"
"v_cvt_pk_bf16_f32 v101, v90, v91\n"
"v_mfma_f32_16x16x32_bf16 v[204:207], a[220:223], a[176:179], v[204:207]\n"
"ds_read_b64_tr_b16 a[206:207], v28 offset:4096\n"
"v_add_f32 v60, 1.0, v56\n"
"v_mfma_f32_16x16x32_bf16 v[220:223], a[220:223], a[180:183], v[220:223]\n"
"v_exp_f32 v52, -v52\n"
"v_mfma_f32_16x16x32_bf16 v[236:239], a[220:223], a[184:187], v[236:239]\n"
"s_add_u32 m0, s62, 4096\n"
"s_add_u32 s61, s61, 0x8000\n"
"v_mfma_f32_16x16x32_bf16 v[252:255], a[220:223], a[188:191], v[252:255]\n"
"s_waitcnt lgkmcnt(4)\n"
"v_fmac_f32 v60, v60, v52\n"
"v_mfma_f32_16x16x32_bf16 v[128:131], a[192:195], a[128:131], v[128:131]\n"
"ds_read_b128 a[160:163], v19 offset:512\n"
"v_mul_f32 v54, s69, v54\n"
"v_mfma_f32_16x16x32_bf16 v[144:147], a[192:195], a[132:135], v[144:147]\n"
"ds_read_b128 a[164:167], v19 offset:2560\n"
"v_accvgpr_read_b32 v51, a23\n"
"v_mfma_f32_16x16x32_bf16 v[160:163], a[192:195], a[136:139], v[160:163]\n"
"v_rcp_f32 v60, v60\n"
"v_mfma_f32_16x16x32_bf16 v[176:179], a[192:195], a[140:143], v[176:179]\n"
"ds_read_b128 a[168:171], v19 offset:4608\n"
"v_fma_f32 v56, -v56, v60, v60\n"
"v_mfma_f32_16x16x32_bf16 v[192:195], a[192:195], a[144:147], v[192:195]\n"
"ds_read_b128 a[172:175], v19 offset:6656\n"
"v_bfi_b32 v56, s70, v56, v48\n"
"v_mfma_f32_16x16x32_bf16 v[208:211], a[192:195], a[148:151], v[208:211]\n"
"s_waitcnt lgkmcnt(4)\n"
"v_accvgpr_read_b32 v55, a31\n"
"v_mfma_f32_16x16x32_bf16 v[224:227], a[192:195], a[152:155], v[224:227]\n"
"buffer_load_dwordx4 v29, s[44:47], s61 offen offset:0 lds\n"
"v_mul_f32 v51, s68, v51\n"
"v_mfma_f32_16x16x32_bf16 v[240:243], a[192:195], a[156:159], v[240:243]\n"
"ds_read_b128 a[176:179], v19 offset:8704\n"
"v_mul_f32 v55, s69, v55\n"
"v_mfma_f32_16x16x32_bf16 v[132:135], a[196:199], a[128:131], v[132:135]\n"
"v_exp_f32 v57, -|v49|\n"
"v_mfma_f32_16x16x32_bf16 v[148:151], a[196:199], a[132:135], v[148:151]\n"
"buffer_load_dwordx4 v30, s[44:47], s61 offen offset:1024 lds\n"
"v_add_f32 v61, 1.0, v57\n"
"v_mfma_f32_16x16x32_bf16 v[164:167], a[196:199], a[136:139], v[164:167]\n"
"ds_read_b128 a[180:183], v19 offset:10752\n"
"v_accvgpr_read_b32 v64, a32\n"
"v_mfma_f32_16x16x32_bf16 v[180:183], a[196:199], a[140:143], v[180:183]\n"
"v_exp_f32 v53, -v53\n"
"v_mfma_f32_16x16x32_bf16 v[196:199], a[196:199], a[144:147], v[196:199]\n"
"buffer_load_dwordx4 v31, s[44:47], s61 offen offset:2048 lds\n"
"v_fmac_f32 v61, v61, v53\n"
"v_mfma_f32_16x16x32_bf16 v[212:215], a[196:199], a[148:151], v[212:215]\n"
"ds_read_b128 a[184:187], v19 offset:12800\n"
"v_accvgpr_read_b32 v68, a40\n"
"v_mfma_f32_16x16x32_bf16 v[228:231], a[196:199], a[152:155], v[228:231]\n"
"v_rcp_f32 v61, v61\n"
"v_mfma_f32_16x16x32_bf16 v[244:247], a[196:199], a[156:159], v[244:247]\n"
"buffer_load_dwordx4 v32, s[44:47], s61 offen offset:3072 lds\n"
"v_fma_f32 v57, -v57, v61, v61\n"
"v_mfma_f32_16x16x32_bf16 v[136:139], a[200:203], a[128:131], v[136:139]\n"
"ds_read_b128 a[188:191], v19 offset:14848\n"
"v_bfi_b32 v57, s70, v57, v49\n"
"v_mfma_f32_16x16x32_bf16 v[152:155], a[200:203], a[132:135], v[152:155]\n"
"s_waitcnt vmcnt(16)\n"
"v_cvt_pk_bf16_f32 v102, v56, v57\n"
"v_mfma_f32_16x16x32_bf16 v[168:171], a[200:203], a[136:139], v[168:171]\n"
"ds_read_b64_tr_b16 a[208:209], v21 offset:8192\n"
"v_add_u32 v19, 17408, v19\n"
"v_mfma_f32_16x16x32_bf16 v[184:187], a[200:203], a[140:143], v[184:187]\n"
"ds_read_b64_tr_b16 a[210:211], v25 offset:8192\n"
"v_mul_f32 v64, s68, v64\n"
"v_mfma_f32_16x16x32_bf16 v[200:203], a[200:203], a[144:147], v[200:203]\n"
"v_exp_f32 v58, -|v50|\n"
"v_mfma_f32_16x16x32_bf16 v[216:219], a[200:203], a[148:151], v[216:219]\n"
"ds_read_b64_tr_b16 a[212:213], v22 offset:8192\n"
"v_add_f32 v62, 1.0, v58\n"
"v_mfma_f32_16x16x32_bf16 v[232:235], a[200:203], a[152:155], v[232:235]\n"
"ds_read_b64_tr_b16 a[214:215], v26 offset:8192\n"
"v_mul_f32 v68, s69, v68\n"
"v_mfma_f32_16x16x32_bf16 v[248:251], a[200:203], a[156:159], v[248:251]\n"
"v_exp_f32 v54, -v54\n"
"v_mfma_f32_16x16x32_bf16 v[140:143], a[204:207], a[128:131], v[140:143]\n"
"ds_read_b64_tr_b16 a[216:217], v23 offset:8192\n"
"v_fmac_f32 v62, v62, v54\n"
"v_mfma_f32_16x16x32_bf16 v[156:159], a[204:207], a[132:135], v[156:159]\n"
"ds_read_b64_tr_b16 a[218:219], v27 offset:8192\n"
"v_accvgpr_read_b32 v65, a33\n"
"v_mfma_f32_16x16x32_bf16 v[172:175], a[204:207], a[136:139], v[172:175]\n"
"v_rcp_f32 v62, v62\n"
"v_mfma_f32_16x16x32_bf16 v[188:191], a[204:207], a[140:143], v[188:191]\n"
"ds_read_b64_tr_b16 a[220:221], v24 offset:8192\n"
"v_fma_f32 v58, -v58, v62, v62\n"
"v_mfma_f32_16x16x32_bf16 v[204:207], a[204:207], a[144:147], v[204:207]\n"
"ds_read_b64_tr_b16 a[222:223], v28 offset:8192\n"
"v_bfi_b32 v58, s70, v58, v50\n"
"v_mfma_f32_16x16x32_bf16 v[220:223], a[204:207], a[148:151], v[220:223]\n"
"v_exp_f32 v59, -|v51|\n"
"v_mfma_f32_16x16x32_bf16 v[236:239], a[204:207], a[152:155], v[236:239]\n"
"s_add_u32 m0, s62, 8192\n"
"s_add_u32 s61, s49, s53\n"
"v_mfma_f32_16x16x32_bf16 v[252:255], a[204:207], a[156:159], v[252:255]\n"
"s_waitcnt lgkmcnt(4)\n"
"v_add_f32 v63, 1.0, v59\n"
"v_mfma_f32_16x16x32_bf16 v[128:131], a[208:211], a[160:163], v[128:131]\n"
"ds_read_b128 a[128:131], v20 offset:0\n"
"v_accvgpr_read_b32 v69, a41\n"
"v_mfma_f32_16x16x32_bf16 v[144:147], a[208:211], a[164:167], v[144:147]\n"
"ds_read_b128 a[132:135], v20 offset:2048\n"
"v_mul_f32 v65, s68, v65\n"
"v_mfma_f32_16x16x32_bf16 v[160:163], a[208:211], a[168:171], v[160:163]\n"
"v_exp_f32 v55, -v55\n"
"v_mfma_f32_16x16x32_bf16 v[176:179], a[208:211], a[172:175], v[176:179]\n"
"ds_read_b128 a[136:139], v20 offset:4096\n"
"v_fmac_f32 v63, v63, v55\n"
"v_mfma_f32_16x16x32_bf16 v[192:195], a[208:211], a[176:179], v[192:195]\n"
"ds_read_b128 a[140:143], v20 offset:6144\n"
"v_mul_f32 v69, s69, v69\n"
"v_mfma_f32_16x16x32_bf16 v[208:211], a[208:211], a[180:183], v[208:211]\n"
"s_waitcnt lgkmcnt(4)\n"
"v_accvgpr_read_b32 v66, a34\n"
"v_mfma_f32_16x16x32_bf16 v[224:227], a[208:211], a[184:187], v[224:227]\n"
"buffer_load_dwordx4 v29, s[44:47], s61 offen offset:0 lds\n"
"v_accvgpr_read_b32 v70, a42\n"
"v_mfma_f32_16x16x32_bf16 v[240:243], a[208:211], a[188:191], v[240:243]\n"
"ds_read_b128 a[144:147], v20 offset:8192\n"
"v_mul_f32 v66, s68, v66\n"
"v_mfma_f32_16x16x32_bf16 v[132:135], a[212:215], a[160:163], v[132:135]\n"
"v_rcp_f32 v63, v63\n"
"v_mfma_f32_16x16x32_bf16 v[148:151], a[212:215], a[164:167], v[148:151]\n"
"buffer_load_dwordx4 v30, s[44:47], s61 offen offset:1024 lds\n"
"v_fma_f32 v59, -v59, v63, v63\n"
"v_mfma_f32_16x16x32_bf16 v[164:167], a[212:215], a[168:171], v[164:167]\n"
"ds_read_b128 a[148:151], v20 offset:10240\n"
"v_bfi_b32 v59, s70, v59, v51\n"
"v_mfma_f32_16x16x32_bf16 v[180:183], a[212:215], a[172:175], v[180:183]\n"
"v_exp_f32 v72, -|v64|\n"
"v_mfma_f32_16x16x32_bf16 v[196:199], a[212:215], a[176:179], v[196:199]\n"
"buffer_load_dwordx4 v31, s[44:47], s61 offen offset:2048 lds\n"
"v_cvt_pk_bf16_f32 v103, v58, v59\n"
"v_mfma_f32_16x16x32_bf16 v[212:215], a[212:215], a[180:183], v[212:215]\n"
"ds_read_b128 a[152:155], v20 offset:12288\n"
"v_add_f32 v76, 1.0, v72\n"
"v_mfma_f32_16x16x32_bf16 v[228:231], a[212:215], a[184:187], v[228:231]\n"
"v_exp_f32 v68, -v68\n"
"v_mfma_f32_16x16x32_bf16 v[244:247], a[212:215], a[188:191], v[244:247]\n"
"buffer_load_dwordx4 v32, s[44:47], s61 offen offset:3072 lds\n"
"v_fmac_f32 v76, v76, v68\n"
"v_mfma_f32_16x16x32_bf16 v[136:139], a[216:219], a[160:163], v[136:139]\n"
"ds_read_b128 a[156:159], v20 offset:14336\n"
"v_mul_f32 v70, s69, v70\n"
"v_mfma_f32_16x16x32_bf16 v[152:155], a[216:219], a[164:167], v[152:155]\n"
"s_waitcnt vmcnt(16)\n"
"v_accvgpr_read_b32 v67, a35\n"
"v_mfma_f32_16x16x32_bf16 v[168:171], a[216:219], a[168:171], v[168:171]\n"
"ds_read_b64_tr_b16 a[192:193], v21 offset:12288\n"
"v_accvgpr_read_b32 v71, a43\n"
"v_mfma_f32_16x16x32_bf16 v[184:187], a[216:219], a[172:175], v[184:187]\n"
"ds_read_b64_tr_b16 a[194:195], v25 offset:12288\n"
"v_mul_f32 v67, s68, v67\n"
"v_mfma_f32_16x16x32_bf16 v[200:203], a[216:219], a[176:179], v[200:203]\n"
"v_rcp_f32 v76, v76\n"
"v_mfma_f32_16x16x32_bf16 v[216:219], a[216:219], a[180:183], v[216:219]\n"
"ds_read_b64_tr_b16 a[196:197], v22 offset:12288\n"
"v_fma_f32 v72, -v72, v76, v76\n"
"v_mfma_f32_16x16x32_bf16 v[232:235], a[216:219], a[184:187], v[232:235]\n"
"ds_read_b64_tr_b16 a[198:199], v26 offset:12288\n"
"v_bfi_b32 v72, s70, v72, v64\n"
"v_mfma_f32_16x16x32_bf16 v[248:251], a[216:219], a[188:191], v[248:251]\n"
"v_exp_f32 v73, -|v65|\n"
"v_mfma_f32_16x16x32_bf16 v[140:143], a[220:223], a[160:163], v[140:143]\n"
"ds_read_b64_tr_b16 a[200:201], v23 offset:12288\n"
"v_add_f32 v77, 1.0, v73\n"
"v_mfma_f32_16x16x32_bf16 v[156:159], a[220:223], a[164:167], v[156:159]\n"
"ds_read_b64_tr_b16 a[202:203], v27 offset:12288\n"
"v_mul_f32 v71, s69, v71\n"
"v_mfma_f32_16x16x32_bf16 v[172:175], a[220:223], a[168:171], v[172:175]\n"
"v_exp_f32 v69, -v69\n"
"v_mfma_f32_16x16x32_bf16 v[188:191], a[220:223], a[172:175], v[188:191]\n"
"ds_read_b64_tr_b16 a[204:205], v24 offset:12288\n"
"v_fmac_f32 v77, v77, v69\n"
"v_mfma_f32_16x16x32_bf16 v[204:207], a[220:223], a[176:179], v[204:207]\n"
"ds_read_b64_tr_b16 a[206:207], v28 offset:12288\n"
"v_accvgpr_read_b32 v80, a36\n"
"v_mfma_f32_16x16x32_bf16 v[220:223], a[220:223], a[180:183], v[220:223]\n"
"v_rcp_f32 v77, v77\n"
"v_mfma_f32_16x16x32_bf16 v[236:239], a[220:223], a[184:187], v[236:239]\n"
"s_add_u32 m0, s62, 12288\n"
"s_add_u32 s61, s61, 0x8000\n"
"v_mfma_f32_16x16x32_bf16 v[252:255], a[220:223], a[188:191], v[252:255]\n"
"s_waitcnt lgkmcnt(4)\n"
"v_fma_f32 v73, -v73, v77, v77\n"
"v_mfma_f32_16x16x32_bf16 v[128:131], a[192:195], a[128:131], v[128:131]\n"
"ds_read_b128 a[160:163], v20 offset:512\n"
"v_bfi_b32 v73, s70, v73, v65\n"
"v_mfma_f32_16x16x32_bf16 v[144:147], a[192:195], a[132:135], v[144:147]\n"
"ds_read_b128 a[164:167], v20 offset:2560\n"
"v_cvt_pk_bf16_f32 v104, v72, v73\n"
"v_mfma_f32_16x16x32_bf16 v[160:163], a[192:195], a[136:139], v[160:163]\n"
"v_exp_f32 v74, -|v66|\n"
"v_mfma_f32_16x16x32_bf16 v[176:179], a[192:195], a[140:143], v[176:179]\n"
"ds_read_b128 a[168:171], v20 offset:4608\n"
"v_add_f32 v78, 1.0, v74\n"
"v_mfma_f32_16x16x32_bf16 v[192:195], a[192:195], a[144:147], v[192:195]\n"
"ds_read_b128 a[172:175], v20 offset:6656\n"
"v_accvgpr_read_b32 v84, a44\n"
"v_mfma_f32_16x16x32_bf16 v[208:211], a[192:195], a[148:151], v[208:211]\n"
"s_waitcnt lgkmcnt(4)\n"
"v_mul_f32 v80, s68, v80\n"
"v_mfma_f32_16x16x32_bf16 v[224:227], a[192:195], a[152:155], v[224:227]\n"
"buffer_load_dwordx4 v29, s[44:47], s61 offen offset:0 lds\n"
"v_mul_f32 v84, s69, v84\n"
"v_mfma_f32_16x16x32_bf16 v[240:243], a[192:195], a[156:159], v[240:243]\n"
"ds_read_b128 a[176:179], v20 offset:8704\n"
"v_accvgpr_read_b32 v81, a37\n"
"v_mfma_f32_16x16x32_bf16 v[132:135], a[196:199], a[128:131], v[132:135]\n"
"v_exp_f32 v70, -v70\n"
"v_mfma_f32_16x16x32_bf16 v[148:151], a[196:199], a[132:135], v[148:151]\n"
"buffer_load_dwordx4 v30, s[44:47], s61 offen offset:1024 lds\n"
"v_fmac_f32 v78, v78, v70\n"
"v_mfma_f32_16x16x32_bf16 v[164:167], a[196:199], a[136:139], v[164:167]\n"
"ds_read_b128 a[180:183], v20 offset:10752\n"
"v_accvgpr_read_b32 v85, a45\n"
"v_mfma_f32_16x16x32_bf16 v[180:183], a[196:199], a[140:143], v[180:183]\n"
"v_rcp_f32 v78, v78\n"
"v_mfma_f32_16x16x32_bf16 v[196:199], a[196:199], a[144:147], v[196:199]\n"
"buffer_load_dwordx4 v31, s[44:47], s61 offen offset:2048 lds\n"
"v_fma_f32 v74, -v74, v78, v78\n"
"v_mfma_f32_16x16x32_bf16 v[212:215], a[196:199], a[148:151], v[212:215]\n"
"ds_read_b128 a[184:187], v20 offset:12800\n"
"v_bfi_b32 v74, s70, v74, v66\n"
"v_mfma_f32_16x16x32_bf16 v[228:231], a[196:199], a[152:155], v[228:231]\n"
"v_exp_f32 v75, -|v67|\n"
"v_mfma_f32_16x16x32_bf16 v[244:247], a[196:199], a[156:159], v[244:247]\n"
"buffer_load_dwordx4 v32, s[44:47], s61 offen offset:3072 lds\n"
"v_add_f32 v79, 1.0, v75\n"
"v_mfma_f32_16x16x32_bf16 v[136:139], a[200:203], a[128:131], v[136:139]\n"
"ds_read_b128 a[188:191], v20 offset:14848\n"
"v_mul_f32 v81, s68, v81\n"
"v_mfma_f32_16x16x32_bf16 v[152:155], a[200:203], a[132:135], v[152:155]\n"
"s_waitcnt vmcnt(16)\n"
"v_mul_f32 v85, s69, v85\n"
"v_mfma_f32_16x16x32_bf16 v[168:171], a[200:203], a[136:139], v[168:171]\n"
"ds_read_b64_tr_b16 a[208:209], v21 offset:16384\n"
"v_add_u32 v20, 17408, v20\n"
"v_mfma_f32_16x16x32_bf16 v[184:187], a[200:203], a[140:143], v[184:187]\n"
"ds_read_b64_tr_b16 a[210:211], v25 offset:16384\n"
"v_accvgpr_read_b32 v82, a38\n"
"v_mfma_f32_16x16x32_bf16 v[200:203], a[200:203], a[144:147], v[200:203]\n"
"v_exp_f32 v71, -v71\n"
"v_mfma_f32_16x16x32_bf16 v[216:219], a[200:203], a[148:151], v[216:219]\n"
"ds_read_b64_tr_b16 a[212:213], v22 offset:16384\n"
"v_fmac_f32 v79, v79, v71\n"
"v_mfma_f32_16x16x32_bf16 v[232:235], a[200:203], a[152:155], v[232:235]\n"
"ds_read_b64_tr_b16 a[214:215], v26 offset:16384\n"
"v_accvgpr_read_b32 v86, a46\n"
"v_mfma_f32_16x16x32_bf16 v[248:251], a[200:203], a[156:159], v[248:251]\n"
"v_rcp_f32 v79, v79\n"
"v_mfma_f32_16x16x32_bf16 v[140:143], a[204:207], a[128:131], v[140:143]\n"
"ds_read_b64_tr_b16 a[216:217], v23 offset:16384\n"
"v_fma_f32 v75, -v75, v79, v79\n"
"v_mfma_f32_16x16x32_bf16 v[156:159], a[204:207], a[132:135], v[156:159]\n"
"ds_read_b64_tr_b16 a[218:219], v27 offset:16384\n"
"v_bfi_b32 v75, s70, v75, v67\n"
"v_mfma_f32_16x16x32_bf16 v[172:175], a[204:207], a[136:139], v[172:175]\n"
"v_exp_f32 v88, -|v80|\n"
"v_mfma_f32_16x16x32_bf16 v[188:191], a[204:207], a[140:143], v[188:191]\n"
"ds_read_b64_tr_b16 a[220:221], v24 offset:16384\n"
"v_cvt_pk_bf16_f32 v105, v74, v75\n"
"v_mfma_f32_16x16x32_bf16 v[204:207], a[204:207], a[144:147], v[204:207]\n"
"ds_read_b64_tr_b16 a[222:223], v28 offset:16384\n"
"v_add_f32 v92, 1.0, v88\n"
"v_mfma_f32_16x16x32_bf16 v[220:223], a[204:207], a[148:151], v[220:223]\n"
"v_exp_f32 v84, -v84\n"
"v_mfma_f32_16x16x32_bf16 v[236:239], a[204:207], a[152:155], v[236:239]\n"
"s_add_u32 m0, s62, 16384\n"
"s_add_u32 s61, s50, s53\n"
"v_mfma_f32_16x16x32_bf16 v[252:255], a[204:207], a[156:159], v[252:255]\n"
"s_waitcnt lgkmcnt(4)\n"
"v_fmac_f32 v92, v92, v84\n"
"v_mfma_f32_16x16x32_bf16 v[128:131], a[208:211], a[160:163], v[128:131]\n"
"ds_read_b128 a[128:131], v16 offset:0\n"
"v_mul_f32 v82, s68, v82\n"
"v_mfma_f32_16x16x32_bf16 v[144:147], a[208:211], a[164:167], v[144:147]\n"
"ds_read_b128 a[132:135], v16 offset:2048\n"
"v_mul_f32 v86, s69, v86\n"
"v_mfma_f32_16x16x32_bf16 v[160:163], a[208:211], a[168:171], v[160:163]\n"
"v_rcp_f32 v92, v92\n"
"v_mfma_f32_16x16x32_bf16 v[176:179], a[208:211], a[172:175], v[176:179]\n"
"ds_read_b128 a[136:139], v16 offset:4096\n"
"v_fma_f32 v88, -v88, v92, v92\n"
"v_mfma_f32_16x16x32_bf16 v[192:195], a[208:211], a[176:179], v[192:195]\n"
"ds_read_b128 a[140:143], v16 offset:6144\n"
"v_bfi_b32 v88, s70, v88, v80\n"
"v_mfma_f32_16x16x32_bf16 v[208:211], a[208:211], a[180:183], v[208:211]\n"
"s_waitcnt lgkmcnt(4)\n"
"v_accvgpr_read_b32 v83, a39\n"
"v_mfma_f32_16x16x32_bf16 v[224:227], a[208:211], a[184:187], v[224:227]\n"
"buffer_load_dwordx4 v29, s[44:47], s61 offen offset:0 lds\n"
"v_accvgpr_read_b32 v87, a47\n"
"v_mfma_f32_16x16x32_bf16 v[240:243], a[208:211], a[188:191], v[240:243]\n"
"ds_read_b128 a[144:147], v16 offset:8192\n"
"v_mul_f32 v83, s68, v83\n"
"v_mfma_f32_16x16x32_bf16 v[132:135], a[212:215], a[160:163], v[132:135]\n"
"v_exp_f32 v89, -|v81|\n"
"v_mfma_f32_16x16x32_bf16 v[148:151], a[212:215], a[164:167], v[148:151]\n"
"buffer_load_dwordx4 v30, s[44:47], s61 offen offset:1024 lds\n"
"v_add_f32 v93, 1.0, v89\n"
"v_mfma_f32_16x16x32_bf16 v[164:167], a[212:215], a[168:171], v[164:167]\n"
"ds_read_b128 a[148:151], v16 offset:10240\n"
"v_mul_f32 v87, s69, v87\n"
"v_mfma_f32_16x16x32_bf16 v[180:183], a[212:215], a[172:175], v[180:183]\n"
"s_barrier\n"
"v_accvgpr_read_b32 v48, a48\n"
"v_mfma_f32_16x16x32_bf16 v[196:199], a[212:215], a[176:179], v[196:199]\n"
"buffer_load_dwordx4 v31, s[44:47], s61 offen offset:2048 lds\n"
"v_accvgpr_read_b32 v52, a56\n"
"v_mfma_f32_16x16x32_bf16 v[212:215], a[212:215], a[180:183], v[212:215]\n"
"ds_read_b128 a[152:155], v16 offset:12288\n"
"v_mul_f32 v48, s68, v48\n"
"v_mfma_f32_16x16x32_bf16 v[228:231], a[212:215], a[184:187], v[228:231]\n"
"v_exp_f32 v85, -v85\n"
"v_mfma_f32_16x16x32_bf16 v[244:247], a[212:215], a[188:191], v[244:247]\n"
"buffer_load_dwordx4 v32, s[44:47], s61 offen offset:3072 lds\n"
"s_xor_b32 s99, s98, 0\n"
"v_mfma_f32_16x16x32_bf16 v[136:139], a[216:219], a[160:163], v[136:139]\n"
"ds_read_b128 a[156:159], v16 offset:14336\n"
"s_add_u32 s99, s99, 0\n"
"v_mfma_f32_16x16x32_bf16 v[152:155], a[216:219], a[164:167], v[152:155]\n"
"s_waitcnt vmcnt(16)\n"
"v_fmac_f32 v93, v93, v85\n"
"v_mfma_f32_16x16x32_bf16 v[168:171], a[216:219], a[168:171], v[168:171]\n"
"ds_read_b64_tr_b16 a[192:193], v21 offset:0\n"
"s_lshl_b32 m0, %[sm0], 2\n"
"v_mfma_f32_16x16x32_bf16 v[184:187], a[216:219], a[172:175], v[184:187]\n"
"ds_read_b64_tr_b16 a[194:195], v25 offset:0\n"
"v_mul_f32 v52, s69, v52\n"
"v_mfma_f32_16x16x32_bf16 v[200:203], a[216:219], a[176:179], v[200:203]\n"
"buffer_load_dwordx4 v33, s[64:67], s99 offen offset:0 lds\n"
"v_accvgpr_read_b32 v49, a49\n"
"v_mfma_f32_16x16x32_bf16 v[216:219], a[216:219], a[180:183], v[216:219]\n"
"ds_read_b64_tr_b16 a[196:197], v22 offset:0\n"
"v_accvgpr_read_b32 v53, a57\n"
"v_mfma_f32_16x16x32_bf16 v[232:235], a[216:219], a[184:187], v[232:235]\n"
"ds_read_b64_tr_b16 a[198:199], v26 offset:0\n"
"v_mul_f32 v49, s68, v49\n"
"v_mfma_f32_16x16x32_bf16 v[248:251], a[216:219], a[188:191], v[248:251]\n"
"buffer_load_dwordx4 v34, s[64:67], s99 offen offset:1024 lds\n"
"v_mul_f32 v53, s69, v53\n"
"v_mfma_f32_16x16x32_bf16 v[140:143], a[220:223], a[160:163], v[140:143]\n"
"ds_read_b64_tr_b16 a[200:201], v23 offset:0\n"
"v_accvgpr_read_b32 v50, a50\n"
"v_mfma_f32_16x16x32_bf16 v[156:159], a[220:223], a[164:167], v[156:159]\n"
"ds_read_b64_tr_b16 a[202:203], v27 offset:0\n"
"v_accvgpr_read_b32 v54, a58\n"
"v_mfma_f32_16x16x32_bf16 v[172:175], a[220:223], a[168:171], v[172:175]\n"
"buffer_load_dwordx4 v35, s[64:67], s99 offen offset:2048 lds\n"
"v_mul_f32 v50, s68, v50\n"
"v_mfma_f32_16x16x32_bf16 v[188:191], a[220:223], a[172:175], v[188:191]\n"
"ds_read_b64_tr_b16 a[204:205], v24 offset:0\n"
"v_mul_f32 v54, s69, v54\n"
"v_mfma_f32_16x16x32_bf16 v[204:207], a[220:223], a[176:179], v[204:207]\n"
"ds_read_b64_tr_b16 a[206:207], v28 offset:0\n"
"v_accvgpr_read_b32 v51, a51\n"
"v_mfma_f32_16x16x32_bf16 v[220:223], a[220:223], a[180:183], v[220:223]\n"
"buffer_load_dwordx4 v36, s[64:67], s99 offen offset:3072 lds\n"
"v_accvgpr_read_b32 v55, a59\n"
"v_mfma_f32_16x16x32_bf16 v[236:239], a[220:223], a[184:187], v[236:239]\n"
"s_add_u32 m0, s62, 0\n"
"s_add_u32 s61, s61, 0x8000\n"
"v_mfma_f32_16x16x32_bf16 v[252:255], a[220:223], a[188:191], v[252:255]\n"
"s_waitcnt lgkmcnt(4)\n"
"v_mul_f32 v51, s68, v51\n"
"v_mfma_f32_16x16x32_bf16 v[128:131], a[192:195], a[128:131], v[128:131]\n"
"ds_read_b128 a[160:163], v16 offset:512\n"
"v_mul_f32 v55, s69, v55\n"
"v_mfma_f32_16x16x32_bf16 v[144:147], a[192:195], a[132:135], v[144:147]\n"
"ds_read_b128 a[164:167], v16 offset:2560\n"
"v_accvgpr_read_b32 v64, a52\n"
"v_mfma_f32_16x16x32_bf16 v[160:163], a[192:195], a[136:139], v[160:163]\n"
"v_rcp_f32 v93, v93\n"
"v_mfma_f32_16x16x32_bf16 v[176:179], a[192:195], a[140:143], v[176:179]\n"
"ds_read_b128 a[168:171], v16 offset:4608\n"
"v_fma_f32 v89, -v89, v93, v93\n"
"v_mfma_f32_16x16x32_bf16 v[192:195], a[192:195], a[144:147], v[192:195]\n"
"ds_read_b128 a[172:175], v16 offset:6656\n"
"v_bfi_b32 v89, s70, v89, v81\n"
"v_mfma_f32_16x16x32_bf16 v[208:211], a[192:195], a[148:151], v[208:211]\n"
"s_waitcnt lgkmcnt(4)\n"
"v_cvt_pk_bf16_f32 v106, v88, v89\n"
"v_mfma_f32_16x16x32_bf16 v[224:227], a[192:195], a[152:155], v[224:227]\n"
"buffer_load_dwordx4 v29, s[44:47], s61 offen offset:0 lds\n"
"v_accvgpr_read_b32 v68, a60\n"
"v_mfma_f32_16x16x32_bf16 v[240:243], a[192:195], a[156:159], v[240:243]\n"
"ds_read_b128 a[176:179], v16 offset:8704\n"
"v_mul_f32 v64, s68, v64\n"
"v_mfma_f32_16x16x32_bf16 v[132:135], a[196:199], a[128:131], v[132:135]\n"
"v_exp_f32 v90, -|v82|\n"
"v_mfma_f32_16x16x32_bf16 v[148:151], a[196:199], a[132:135], v[148:151]\n"
"buffer_load_dwordx4 v30, s[44:47], s61 offen offset:1024 lds\n"
"v_add_f32 v94, 1.0, v90\n"
"v_mfma_f32_16x16x32_bf16 v[164:167], a[196:199], a[136:139], v[164:167]\n"
"ds_read_b128 a[180:183], v16 offset:10752\n"
"v_mul_f32 v68, s69, v68\n"
"v_mfma_f32_16x16x32_bf16 v[180:183], a[196:199], a[140:143], v[180:183]\n"
"v_exp_f32 v86, -v86\n"
"v_mfma_f32_16x16x32_bf16 v[196:199], a[196:199], a[144:147], v[196:199]\n"
"buffer_load_dwordx4 v31, s[44:47], s61 offen offset:2048 lds\n"
"v_fmac_f32 v94, v94, v86\n"
"v_mfma_f32_16x16x32_bf16 v[212:215], a[196:199], a[148:151], v[212:215]\n"
"ds_read_b128 a[184:187], v16 offset:12800\n"
"v_accvgpr_read_b32 v65, a53\n"
"v_mfma_f32_16x16x32_bf16 v[228:231], a[196:199], a[152:155], v[228:231]\n"
"v_rcp_f32 v94, v94\n"
"v_mfma_f32_16x16x32_bf16 v[244:247], a[196:199], a[156:159], v[244:247]\n"
"buffer_load_dwordx4 v32, s[44:47], s61 offen offset:3072 lds\n"
"v_fma_f32 v90, -v90, v94, v94\n"
"v_mfma_f32_16x16x32_bf16 v[136:139], a[200:203], a[128:131], v[136:139]\n"
"ds_read_b128 a[188:191], v16 offset:14848\n"
"v_bfi_b32 v90, s70, v90, v82\n"
"v_mfma_f32_16x16x32_bf16 v[152:155], a[200:203], a[132:135], v[152:155]\n"
"s_waitcnt vmcnt(20)\n"
"v_accvgpr_read_b32 v69, a61\n"
"v_mfma_f32_16x16x32_bf16 v[168:171], a[200:203], a[136:139], v[168:171]\n"
"ds_read_b64_tr_b16 a[208:209], v21 offset:4096\n"
"v_add_u32 v16, 17408, v16\n"
"v_mfma_f32_16x16x32_bf16 v[184:187], a[200:203], a[140:143], v[184:187]\n"
"ds_read_b64_tr_b16 a[210:211], v25 offset:4096\n"
"v_mul_f32 v65, s68, v65\n"
"v_mfma_f32_16x16x32_bf16 v[200:203], a[200:203], a[144:147], v[200:203]\n"
"v_exp_f32 v91, -|v83|\n"
"v_mfma_f32_16x16x32_bf16 v[216:219], a[200:203], a[148:151], v[216:219]\n"
"ds_read_b64_tr_b16 a[212:213], v22 offset:4096\n"
"v_add_f32 v95, 1.0, v91\n"
"v_mfma_f32_16x16x32_bf16 v[232:235], a[200:203], a[152:155], v[232:235]\n"
"ds_read_b64_tr_b16 a[214:215], v26 offset:4096\n"
"v_mul_f32 v69, s69, v69\n"
"v_mfma_f32_16x16x32_bf16 v[248:251], a[200:203], a[156:159], v[248:251]\n"
"v_exp_f32 v87, -v87\n"
"v_mfma_f32_16x16x32_bf16 v[140:143], a[204:207], a[128:131], v[140:143]\n"
"ds_read_b64_tr_b16 a[216:217], v23 offset:4096\n"
"v_fmac_f32 v95, v95, v87\n"
"v_mfma_f32_16x16x32_bf16 v[156:159], a[204:207], a[132:135], v[156:159]\n"
"ds_read_b64_tr_b16 a[218:219], v27 offset:4096\n"
"v_accvgpr_read_b32 v66, a54\n"
"v_mfma_f32_16x16x32_bf16 v[172:175], a[204:207], a[136:139], v[172:175]\n"
"v_rcp_f32 v95, v95\n"
"v_mfma_f32_16x16x32_bf16 v[188:191], a[204:207], a[140:143], v[188:191]\n"
"ds_read_b64_tr_b16 a[220:221], v24 offset:4096\n"
"v_fma_f32 v91, -v91, v95, v95\n"
"v_mfma_f32_16x16x32_bf16 v[204:207], a[204:207], a[144:147], v[204:207]\n"
"ds_read_b64_tr_b16 a[222:223], v28 offset:4096\n"
"v_bfi_b32 v91, s70, v91, v83\n"
"v_mfma_f32_16x16x32_bf16 v[220:223], a[204:207], a[148:151], v[220:223]\n"
"v_exp_f32 v56, -|v48|\n"
"v_mfma_f32_16x16x32_bf16 v[236:239], a[204:207], a[152:155], v[236:239]\n"
"s_add_u32 m0, s62, 4096\n"
"s_add_u32 s61, s51, s53\n"
"v_mfma_f32_16x16x32_bf16 v[252:255], a[204:207], a[156:159], v[252:255]\n"
"s_waitcnt lgkmcnt(4)\n"
"v_cvt_pk_bf16_f32 v107, v90, v91\n"
"v_mfma_f32_16x16x32_bf16 v[128:131], a[208:211], a[160:163], v[128:131]\n"
"ds_read_b128 a[128:131], v17 offset:0\n"
"v_add_f32 v60, 1.0, v56\n"
"v_mfma_f32_16x16x32_bf16 v[144:147], a[208:211], a[164:167], v[144:147]\n"
"ds_read_b128 a[132:135], v17 offset:2048\n"
"v_accvgpr_read_b32 v70, a62\n"
"v_mfma_f32_16x16x32_bf16 v[160:163], a[208:211], a[168:171], v[160:163]\n"
"v_exp_f32 v52, -v52\n"
"v_mfma_f32_16x16x32_bf16 v[176:179], a[208:211], a[172:175], v[176:179]\n"
"ds_read_b128 a[136:139], v17 offset:4096\n"
"v_fmac_f32 v60, v60, v52\n"
"v_mfma_f32_16x16x32_bf16 v[192:195], a[208:211], a[176:179], v[192:195]\n"
"ds_read_b128 a[140:143], v17 offset:6144\n"
"v_mul_f32 v66, s68, v66\n"
"v_mfma_f32_16x16x32_bf16 v[208:211], a[208:211], a[180:183], v[208:211]\n"
"s_waitcnt lgkmcnt(4)\n"
"v_mul_f32 v70, s69, v70\n"
"v_mfma_f32_16x16x32_bf16 v[224:227], a[208:211], a[184:187], v[224:227]\n"
"buffer_load_dwordx4 v29, s[44:47], s61 offen offset:0 lds\n"
"v_accvgpr_read_b32 v67, a55\n"
"v_mfma_f32_16x16x32_bf16 v[240:243], a[208:211], a[188:191], v[240:243]\n"
"ds_read_b128 a[144:147], v17 offset:8192\n"
"v_accvgpr_read_b32 v71, a63\n"
"v_mfma_f32_16x16x32_bf16 v[132:135], a[212:215], a[160:163], v[132:135]\n"
"v_rcp_f32 v60, v60\n"
"v_mfma_f32_16x16x32_bf16 v[148:151], a[212:215], a[164:167], v[148:151]\n"
"buffer_load_dwordx4 v30, s[44:47], s61 offen offset:1024 lds\n"
"v_fma_f32 v56, -v56, v60, v60\n"
"v_mfma_f32_16x16x32_bf16 v[164:167], a[212:215], a[168:171], v[164:167]\n"
"ds_read_b128 a[148:151], v17 offset:10240\n"
"v_bfi_b32 v56, s70, v56, v48\n"
"v_mfma_f32_16x16x32_bf16 v[180:183], a[212:215], a[172:175], v[180:183]\n"
"v_exp_f32 v57, -|v49|\n"
"v_mfma_f32_16x16x32_bf16 v[196:199], a[212:215], a[176:179], v[196:199]\n"
"buffer_load_dwordx4 v31, s[44:47], s61 offen offset:2048 lds\n"
"v_add_f32 v61, 1.0, v57\n"
"v_mfma_f32_16x16x32_bf16 v[212:215], a[212:215], a[180:183], v[212:215]\n"
"ds_read_b128 a[152:155], v17 offset:12288\n"
"v_mul_f32 v67, s68, v67\n"
"v_mfma_f32_16x16x32_bf16 v[228:231], a[212:215], a[184:187], v[228:231]\n"
"v_exp_f32 v53, -v53\n"
"v_mfma_f32_16x16x32_bf16 v[244:247], a[212:215], a[188:191], v[244:247]\n"
"buffer_load_dwordx4 v32, s[44:47], s61 offen offset:3072 lds\n"
"v_fmac_f32 v61, v61, v53\n"
"v_mfma_f32_16x16x32_bf16 v[136:139], a[216:219], a[160:163], v[136:139]\n"
"ds_read_b128 a[156:159], v17 offset:14336\n"
"v_mul_f32 v71, s69, v71\n"
"v_mfma_f32_16x16x32_bf16 v[152:155], a[216:219], a[164:167], v[152:155]\n"
"s_waitcnt vmcnt(20)\n"
"v_accvgpr_read_b32 v80, a64\n"
"v_mfma_f32_16x16x32_bf16 v[168:171], a[216:219], a[168:171], v[168:171]\n"
"ds_read_b64_tr_b16 a[192:193], v21 offset:8192\n"
"v_accvgpr_read_b32 v84, a72\n"
"v_mfma_f32_16x16x32_bf16 v[184:187], a[216:219], a[172:175], v[184:187]\n"
"ds_read_b64_tr_b16 a[194:195], v25 offset:8192\n"
"v_mul_f32 v80, s68, v80\n"
"v_mfma_f32_16x16x32_bf16 v[200:203], a[216:219], a[176:179], v[200:203]\n"
"v_rcp_f32 v61, v61\n"
"v_mfma_f32_16x16x32_bf16 v[216:219], a[216:219], a[180:183], v[216:219]\n"
"ds_read_b64_tr_b16 a[196:197], v22 offset:8192\n"
"v_fma_f32 v57, -v57, v61, v61\n"
"v_mfma_f32_16x16x32_bf16 v[232:235], a[216:219], a[184:187], v[232:235]\n"
"ds_read_b64_tr_b16 a[198:199], v26 offset:8192\n"
"v_bfi_b32 v57, s70, v57, v49\n"
"v_mfma_f32_16x16x32_bf16 v[248:251], a[216:219], a[188:191], v[248:251]\n"
"v_exp_f32 v58, -|v50|\n"
"v_mfma_f32_16x16x32_bf16 v[140:143], a[220:223], a[160:163], v[140:143]\n"
"ds_read_b64_tr_b16 a[200:201], v23 offset:8192\n"
"v_add_f32 v62, 1.0, v58\n"
"v_mfma_f32_16x16x32_bf16 v[156:159], a[220:223], a[164:167], v[156:159]\n"
"ds_read_b64_tr_b16 a[202:203], v27 offset:8192\n"
"v_cvt_pk_bf16_f32 v108, v56, v57\n"
"v_mfma_f32_16x16x32_bf16 v[172:175], a[220:223], a[168:171], v[172:175]\n"
"v_exp_f32 v54, -v54\n"
"v_mfma_f32_16x16x32_bf16 v[188:191], a[220:223], a[172:175], v[188:191]\n"
"ds_read_b64_tr_b16 a[204:205], v24 offset:8192\n"
"v_fmac_f32 v62, v62, v54\n"
"v_mfma_f32_16x16x32_bf16 v[204:207], a[220:223], a[176:179], v[204:207]\n"
"ds_read_b64_tr_b16 a[206:207], v28 offset:8192\n"
"v_mul_f32 v84, s69, v84\n"
"v_mfma_f32_16x16x32_bf16 v[220:223], a[220:223], a[180:183], v[220:223]\n"
"v_rcp_f32 v62, v62\n"
"v_mfma_f32_16x16x32_bf16 v[236:239], a[220:223], a[184:187], v[236:239]\n"
"s_add_u32 m0, s62, 8192\n"
"s_add_u32 s61, s61, 0x8000\n"
"v_mfma_f32_16x16x32_bf16 v[252:255], a[220:223], a[188:191], v[252:255]\n"
"s_waitcnt lgkmcnt(4)\n"
"v_fma_f32 v58, -v58, v62, v62\n"
"v_mfma_f32_16x16x32_bf16 v[128:131], a[192:195], a[128:131], v[128:131]\n"
"ds_read_b128 a[160:163], v17 offset:512\n"
"v_bfi_b32 v58, s70, v58, v50\n"
"v_mfma_f32_16x16x32_bf16 v[144:147], a[192:195], a[132:135], v[144:147]\n"
"ds_read_b128 a[164:167], v17 offset:2560\n"
"v_accvgpr_read_b32 v81, a65\n"
"v_mfma_f32_16x16x32_bf16 v[160:163], a[192:195], a[136:139], v[160:163]\n"
"v_exp_f32 v59, -|v51|\n"
"v_mfma_f32_16x16x32_bf16 v[176:179], a[192:195], a[140:143], v[176:179]\n"
"ds_read_b128 a[168:171], v17 offset:4608\n"
"v_add_f32 v63, 1.0, v59\n"
"v_mfma_f32_16x16x32_bf16 v[192:195], a[192:195], a[144:147], v[192:195]\n"
"ds_read_b128 a[172:175], v17 offset:6656\n"
"v_accvgpr_read_b32 v85, a73\n"
"v_mfma_f32_16x16x32_bf16 v[208:211], a[192:195], a[148:151], v[208:211]\n"
"s_waitcnt lgkmcnt(4)\n"
"v_mul_f32 v81, s68, v81\n"
"v_mfma_f32_16x16x32_bf16 v[224:227], a[192:195], a[152:155], v[224:227]\n"
"buffer_load_dwordx4 v29, s[44:47], s61 offen offset:0 lds\n"
"v_mul_f32 v85, s69, v85\n"
"v_mfma_f32_16x16x32_bf16 v[240:243], a[192:195], a[156:159], v[240:243]\n"
"ds_read_b128 a[176:179], v17 offset:8704\n"
"v_accvgpr_read_b32 v82, a66\n"
"v_mfma_f32_16x16x32_bf16 v[132:135], a[196:199], a[128:131], v[132:135]\n"
"v_exp_f32 v55, -v55\n"
"v_mfma_f32_16x16x32_bf16 v[148:151], a[196:199], a[132:135], v[148:151]\n"
"buffer_load_dwordx4 v30, s[44:47], s61 offen offset:1024 lds\n"
"v_fmac_f32 v63, v63, v55\n"
"v_mfma_f32_16x16x32_bf16 v[164:167], a[196:199], a[136:139], v[164:167]\n"
"ds_read_b128 a[180:183], v17 offset:10752\n"
"v_accvgpr_read_b32 v86, a74\n"
"v_mfma_f32_16x16x32_bf16 v[180:183], a[196:199], a[140:143], v[180:183]\n"
"v_rcp_f32 v63, v63\n"
"v_mfma_f32_16x16x32_bf16 v[196:199], a[196:199], a[144:147], v[196:199]\n"
"buffer_load_dwordx4 v31, s[44:47], s61 offen offset:2048 lds\n"
"v_fma_f32 v59, -v59, v63, v63\n"
"v_mfma_f32_16x16x32_bf16 v[212:215], a[196:199], a[148:151], v[212:215]\n"
"ds_read_b128 a[184:187], v17 offset:12800\n"
"v_bfi_b32 v59, s70, v59, v51\n"
"v_mfma_f32_16x16x32_bf16 v[228:231], a[196:199], a[152:155], v[228:231]\n"
"v_exp_f32 v72, -|v64|\n"
"v_mfma_f32_16x16x32_bf16 v[244:247], a[196:199], a[156:159], v[244:247]\n"
"buffer_load_dwordx4 v32, s[44:47], s61 offen offset:3072 lds\n"
"v_cvt_pk_bf16_f32 v109, v58, v59\n"
"v_mfma_f32_16x16x32_bf16 v[136:139], a[200:203], a[128:131], v[136:139]\n"
"ds_read_b128 a[188:191], v17 offset:14848\n"
"v_add_f32 v76, 1.0, v72\n"
"v_mfma_f32_16x16x32_bf16 v[152:155], a[200:203], a[132:135], v[152:155]\n"
"s_waitcnt vmcnt(12)\n"
"v_mul_f32 v82, s68, v82\n"
"v_mfma_f32_16x16x32_bf16 v[168:171], a[200:203], a[136:139], v[168:171]\n"
"ds_read_b64_tr_b16 a[208:209], v21 offset:12288\n"
"v_add_u32 v17, 17408, v17\n"
"v_mfma_f32_16x16x32_bf16 v[184:187], a[200:203], a[140:143], v[184:187]\n"
"ds_read_b64_tr_b16 a[210:211], v25 offset:12288\n"
"v_mul_f32 v86, s69, v86\n"
"v_mfma_f32_16x16x32_bf16 v[200:203], a[200:203], a[144:147], v[200:203]\n"
"v_exp_f32 v68, -v68\n"
"v_mfma_f32_16x16x32_bf16 v[216:219], a[200:203], a[148:151], v[216:219]\n"
"ds_read_b64_tr_b16 a[212:213], v22 offset:12288\n"
"v_fmac_f32 v76, v76, v68\n"
"v_mfma_f32_16x16x32_bf16 v[232:235], a[200:203], a[152:155], v[232:235]\n"
"ds_read_b64_tr_b16 a[214:215], v26 offset:12288\n"
"v_accvgpr_read_b32 v83, a67\n"
"v_mfma_f32_16x16x32_bf16 v[248:251], a[200:203], a[156:159], v[248:251]\n"
"v_rcp_f32 v76, v76\n"
"v_mfma_f32_16x16x32_bf16 v[140:143], a[204:207], a[128:131], v[140:143]\n"
"ds_read_b64_tr_b16 a[216:217], v23 offset:12288\n"
"v_fma_f32 v72, -v72, v76, v76\n"
"v_mfma_f32_16x16x32_bf16 v[156:159], a[204:207], a[132:135], v[156:159]\n"
"ds_read_b64_tr_b16 a[218:219], v27 offset:12288\n"
"v_bfi_b32 v72, s70, v72, v64\n"
"v_mfma_f32_16x16x32_bf16 v[172:175], a[204:207], a[136:139], v[172:175]\n"
"v_exp_f32 v73, -|v65|\n"
"v_mfma_f32_16x16x32_bf16 v[188:191], a[204:207], a[140:143], v[188:191]\n"
"ds_read_b64_tr_b16 a[220:221], v24 offset:12288\n"
"v_add_f32 v77, 1.0, v73\n"
"v_mfma_f32_16x16x32_bf16 v[204:207], a[204:207], a[144:147], v[204:207]\n"
"ds_read_b64_tr_b16 a[222:223], v28 offset:12288\n"
"v_accvgpr_read_b32 v87, a75\n"
"v_mfma_f32_16x16x32_bf16 v[220:223], a[204:207], a[148:151], v[220:223]\n"
"v_exp_f32 v69, -v69\n"
"v_mfma_f32_16x16x32_bf16 v[236:239], a[204:207], a[152:155], v[236:239]\n"
"s_add_u32 m0, s62, 12288\n"
"s_add_u32 s61, s52, s53\n"
"v_mfma_f32_16x16x32_bf16 v[252:255], a[204:207], a[156:159], v[252:255]\n"
"s_waitcnt lgkmcnt(4)\n"
"v_fmac_f32 v77, v77, v69\n"
"v_mfma_f32_16x16x32_bf16 v[128:131], a[208:211], a[160:163], v[128:131]\n"
"ds_read_b128 a[128:131], v18 offset:0\n"
"v_mul_f32 v83, s68, v83\n"
"v_mfma_f32_16x16x32_bf16 v[144:147], a[208:211], a[164:167], v[144:147]\n"
"ds_read_b128 a[132:135], v18 offset:2048\n"
"v_mul_f32 v87, s69, v87\n"
"v_mfma_f32_16x16x32_bf16 v[160:163], a[208:211], a[168:171], v[160:163]\n"
"ds_read_b64_tr_b16 a[0:1], v37 offset:0\n"
"ds_read_b64_tr_b16 a[2:3], v39 offset:0\n"
"v_mfma_f32_16x16x32_bf16 v[176:179], a[208:211], a[172:175], v[176:179]\n"
"ds_read_b128 a[136:139], v18 offset:4096\n"
"v_accvgpr_read_b32 v48, a68\n"
"v_mfma_f32_16x16x32_bf16 v[192:195], a[208:211], a[176:179], v[192:195]\n"
"ds_read_b128 a[140:143], v18 offset:6144\n"
"v_accvgpr_read_b32 v52, a76\n"
"v_mfma_f32_16x16x32_bf16 v[208:211], a[208:211], a[180:183], v[208:211]\n"
"s_waitcnt lgkmcnt(6)\n"
"v_mul_f32 v48, s68, v48\n"
"v_mfma_f32_16x16x32_bf16 v[224:227], a[208:211], a[184:187], v[224:227]\n"
"buffer_load_dwordx4 v29, s[44:47], s61 offen offset:0 lds\n"
"v_mul_f32 v52, s69, v52\n"
"v_mfma_f32_16x16x32_bf16 v[240:243], a[208:211], a[188:191], v[240:243]\n"
"ds_read_b128 a[144:147], v18 offset:8192\n"
"v_accvgpr_read_b32 v49, a69\n"
"v_mfma_f32_16x16x32_bf16 v[132:135], a[212:215], a[160:163], v[132:135]\n"
"ds_read_b64_tr_b16 a[4:5], v38 offset:0\n"
"ds_read_b64_tr_b16 a[6:7], v40 offset:0\n"
"v_mfma_f32_16x16x32_bf16 v[148:151], a[212:215], a[164:167], v[148:151]\n"
"buffer_load_dwordx4 v30, s[44:47], s61 offen offset:1024 lds\n"
"v_accvgpr_read_b32 v53, a77\n"
"v_mfma_f32_16x16x32_bf16 v[164:167], a[212:215], a[168:171], v[164:167]\n"
"ds_read_b128 a[148:151], v18 offset:10240\n"
"v_mul_f32 v49, s68, v49\n"
"v_mfma_f32_16x16x32_bf16 v[180:183], a[212:215], a[172:175], v[180:183]\n"
"ds_read_b64_tr_b16 a[8:9], v37 offset:512\n"
"ds_read_b64_tr_b16 a[10:11], v39 offset:512\n"
"v_mfma_f32_16x16x32_bf16 v[196:199], a[212:215], a[176:179], v[196:199]\n"
"buffer_load_dwordx4 v31, s[44:47], s61 offen offset:2048 lds\n"
"v_mul_f32 v53, s69, v53\n"
"v_mfma_f32_16x16x32_bf16 v[212:215], a[212:215], a[180:183], v[212:215]\n"
"ds_read_b128 a[152:155], v18 offset:12288\n"
"v_accvgpr_read_b32 v50, a70\n"
"v_mfma_f32_16x16x32_bf16 v[228:231], a[212:215], a[184:187], v[228:231]\n"
"ds_read_b64_tr_b16 a[12:13], v38 offset:512\n"
"ds_read_b64_tr_b16 a[14:15], v40 offset:512\n"
"v_mfma_f32_16x16x32_bf16 v[244:247], a[212:215], a[188:191], v[244:247]\n"
"buffer_load_dwordx4 v32, s[44:47], s61 offen offset:3072 lds\n"
"s_xor_b32 s99, s98, 0\n"
"v_mfma_f32_16x16x32_bf16 v[136:139], a[216:219], a[160:163], v[136:139]\n"
"ds_read_b128 a[156:159], v18 offset:14336\n"
"s_add_u32 s99, s99, 16384\n"
"v_mfma_f32_16x16x32_bf16 v[152:155], a[216:219], a[164:167], v[152:155]\n"
"s_waitcnt vmcnt(20)\n"
"v_accvgpr_read_b32 v54, a78\n"
"v_mfma_f32_16x16x32_bf16 v[168:171], a[216:219], a[168:171], v[168:171]\n"
"ds_read_b64_tr_b16 a[192:193], v21 offset:16384\n"
"s_lshl_b32 m0, %[sm0], 2\n"
"v_mfma_f32_16x16x32_bf16 v[184:187], a[216:219], a[172:175], v[184:187]\n"
"ds_read_b64_tr_b16 a[194:195], v25 offset:16384\n"
"s_waitcnt lgkmcnt(3)\n"
"v_mfma_f32_16x16x32_bf16 v[200:203], a[216:219], a[176:179], v[200:203]\n"
"buffer_load_dwordx4 v33, s[64:67], s99 offen offset:0 lds\n"
"s_add_u32 s53, s53, 0x10000\n"
"s_and_b32 s53, s53, 0x3ffff\n"
"v_mfma_f32_16x16x32_bf16 v[216:219], a[216:219], a[180:183], v[216:219]\n"
"ds_read_b64_tr_b16 a[196:197], v22 offset:16384\n"
"v_mul_f32 v50, s68, v50\n"
"v_mfma_f32_16x16x32_bf16 v[232:235], a[216:219], a[184:187], v[232:235]\n"
"ds_read_b64_tr_b16 a[198:199], v26 offset:16384\n"
"v_mul_f32 v54, s69, v54\n"
"v_mfma_f32_16x16x32_bf16 v[248:251], a[216:219], a[188:191], v[248:251]\n"
"buffer_load_dwordx4 v34, s[64:67], s99 offen offset:1024 lds\n"
"v_accvgpr_read_b32 v51, a71\n"
"v_mfma_f32_16x16x32_bf16 v[140:143], a[220:223], a[160:163], v[140:143]\n"
"ds_read_b64_tr_b16 a[200:201], v23 offset:16384\n"
"v_accvgpr_read_b32 v55, a79\n"
"v_mfma_f32_16x16x32_bf16 v[156:159], a[220:223], a[164:167], v[156:159]\n"
"ds_read_b64_tr_b16 a[202:203], v27 offset:16384\n"
"v_mul_f32 v51, s68, v51\n"
"v_mfma_f32_16x16x32_bf16 v[172:175], a[220:223], a[168:171], v[172:175]\n"
"buffer_load_dwordx4 v35, s[64:67], s99 offen offset:2048 lds\n"
"v_mul_f32 v55, s69, v55\n"
"v_mfma_f32_16x16x32_bf16 v[188:191], a[220:223], a[172:175], v[188:191]\n"
"ds_read_b64_tr_b16 a[204:205], v24 offset:16384\n"
"v_mfma_f32_16x16x32_bf16 v[204:207], a[220:223], a[176:179], v[204:207]\n"
"ds_read_b64_tr_b16 a[206:207], v28 offset:16384\n"
"v_mfma_f32_16x16x32_bf16 v[220:223], a[220:223], a[180:183], v[220:223]\n"
"buffer_load_dwordx4 v36, s[64:67], s99 offen offset:3072 lds\n"
"v_mfma_f32_16x16x32_bf16 v[236:239], a[220:223], a[184:187], v[236:239]\n"
"s_add_u32 m0, s62, 16384\n"
"s_add_u32 s61, s61, 0x8000\n"
"v_mfma_f32_16x16x32_bf16 v[252:255], a[220:223], a[188:191], v[252:255]\n"
"s_waitcnt lgkmcnt(4)\n"
"v_mfma_f32_16x16x32_bf16 v[128:131], a[192:195], a[128:131], v[128:131]\n"
"ds_read_b128 a[160:163], v18 offset:512\n"
"v_mfma_f32_16x16x32_bf16 v[144:147], a[192:195], a[132:135], v[144:147]\n"
"ds_read_b128 a[164:167], v18 offset:2560\n"
"v_mfma_f32_16x16x32_bf16 v[160:163], a[192:195], a[136:139], v[160:163]\n"
"v_rcp_f32 v77, v77\n"
"v_mfma_f32_16x16x32_bf16 v[176:179], a[192:195], a[140:143], v[176:179]\n"
"ds_read_b128 a[168:171], v18 offset:4608\n"
"v_fma_f32 v73, -v73, v77, v77\n"
"v_mfma_f32_16x16x32_bf16 v[192:195], a[192:195], a[144:147], v[192:195]\n"
"ds_read_b128 a[172:175], v18 offset:6656\n"
"v_bfi_b32 v73, s70, v73, v65\n"
"v_mfma_f32_16x16x32_bf16 v[208:211], a[192:195], a[148:151], v[208:211]\n"
"s_waitcnt lgkmcnt(4)\n"
"v_cvt_pk_bf16_f32 v110, v72, v73\n"
"v_mfma_f32_16x16x32_bf16 v[224:227], a[192:195], a[152:155], v[224:227]\n"
"buffer_load_dwordx4 v29, s[44:47], s61 offen offset:0 lds\n"
"v_mfma_f32_16x16x32_bf16 v[240:243], a[192:195], a[156:159], v[240:243]\n"
"ds_read_b128 a[176:179], v18 offset:8704\n"
"v_mfma_f32_16x16x32_bf16 v[132:135], a[196:199], a[128:131], v[132:135]\n"
"v_exp_f32 v74, -|v66|\n"
"v_mfma_f32_16x16x32_bf16 v[148:151], a[196:199], a[132:135], v[148:151]\n"
"buffer_load_dwordx4 v30, s[44:47], s61 offen offset:1024 lds\n"
"v_add_f32 v78, 1.0, v74\n"
"v_mfma_f32_16x16x32_bf16 v[164:167], a[196:199], a[136:139], v[164:167]\n"
"ds_read_b128 a[180:183], v18 offset:10752\n"
"v_mfma_f32_16x16x32_bf16 v[180:183], a[196:199], a[140:143], v[180:183]\n"
"v_exp_f32 v70, -v70\n"
"v_mfma_f32_16x16x32_bf16 v[196:199], a[196:199], a[144:147], v[196:199]\n"
"buffer_load_dwordx4 v31, s[44:47], s61 offen offset:2048 lds\n"
"v_fmac_f32 v78, v78, v70\n"
"v_mfma_f32_16x16x32_bf16 v[212:215], a[196:199], a[148:151], v[212:215]\n"
"ds_read_b128 a[184:187], v18 offset:12800\n"
"v_mfma_f32_16x16x32_bf16 v[228:231], a[196:199], a[152:155], v[228:231]\n"
"v_rcp_f32 v78, v78\n"
"v_mfma_f32_16x16x32_bf16 v[244:247], a[196:199], a[156:159], v[244:247]\n"
"buffer_load_dwordx4 v32, s[44:47], s61 offen offset:3072 lds\n"
"v_fma_f32 v74, -v74, v78, v78\n"
"v_mfma_f32_16x16x32_bf16 v[136:139], a[200:203], a[128:131], v[136:139]\n"
"ds_read_b128 a[188:191], v18 offset:14848\n"
"v_bfi_b32 v74, s70, v74, v66\n"
"v_mfma_f32_16x16x32_bf16 v[152:155], a[200:203], a[132:135], v[152:155]\n"
"s_waitcnt vmcnt(20)\n"
"v_mfma_f32_16x16x32_bf16 v[168:171], a[200:203], a[136:139], v[168:171]\n"
"ds_read_b64_tr_b16 a[208:209], v21 offset:0\n"
"v_add_u32 v18, 17408, v18\n"
"v_mfma_f32_16x16x32_bf16 v[184:187], a[200:203], a[140:143], v[184:187]\n"
"ds_read_b64_tr_b16 a[210:211], v25 offset:0\n"
"v_mfma_f32_16x16x32_bf16 v[200:203], a[200:203], a[144:147], v[200:203]\n"
"v_exp_f32 v75, -|v67|\n"
"v_mfma_f32_16x16x32_bf16 v[216:219], a[200:203], a[148:151], v[216:219]\n"
"ds_read_b64_tr_b16 a[212:213], v22 offset:0\n"
"v_add_f32 v79, 1.0, v75\n"
"v_mfma_f32_16x16x32_bf16 v[232:235], a[200:203], a[152:155], v[232:235]\n"
"ds_read_b64_tr_b16 a[214:215], v26 offset:0\n"
"v_mfma_f32_16x16x32_bf16 v[248:251], a[200:203], a[156:159], v[248:251]\n"
"v_exp_f32 v71, -v71\n"
"v_mfma_f32_16x16x32_bf16 v[140:143], a[204:207], a[128:131], v[140:143]\n"
"ds_read_b64_tr_b16 a[216:217], v23 offset:0\n"
"v_fmac_f32 v79, v79, v71\n"
"v_mfma_f32_16x16x32_bf16 v[156:159], a[204:207], a[132:135], v[156:159]\n"
"ds_read_b64_tr_b16 a[218:219], v27 offset:0\n"
"v_mfma_f32_16x16x32_bf16 v[172:175], a[204:207], a[136:139], v[172:175]\n"
"v_rcp_f32 v79, v79\n"
"v_mfma_f32_16x16x32_bf16 v[188:191], a[204:207], a[140:143], v[188:191]\n"
"ds_read_b64_tr_b16 a[220:221], v24 offset:0\n"
"v_fma_f32 v75, -v75, v79, v79\n"
"v_mfma_f32_16x16x32_bf16 v[204:207], a[204:207], a[144:147], v[204:207]\n"
"ds_read_b64_tr_b16 a[222:223], v28 offset:0\n"
"v_bfi_b32 v75, s70, v75, v67\n"
"v_mfma_f32_16x16x32_bf16 v[220:223], a[204:207], a[148:151], v[220:223]\n"
"v_exp_f32 v88, -|v80|\n"
"v_mfma_f32_16x16x32_bf16 v[236:239], a[204:207], a[152:155], v[236:239]\n"
"s_add_u32 m0, s62, 0\n"
"s_add_u32 s61, s48, s53\n"
"v_mfma_f32_16x16x32_bf16 v[252:255], a[204:207], a[156:159], v[252:255]\n"
"s_waitcnt lgkmcnt(4)\n"
"v_cvt_pk_bf16_f32 v111, v74, v75\n"
"v_mfma_f32_16x16x32_bf16 v[128:131], a[208:211], a[160:163], v[128:131]\n"
"ds_read_b128 a[128:131], v19 offset:0\n"
"v_add_f32 v92, 1.0, v88\n"
"v_mfma_f32_16x16x32_bf16 v[144:147], a[208:211], a[164:167], v[144:147]\n"
"ds_read_b128 a[132:135], v19 offset:2048\n"
"v_accvgpr_read_b32 v64, a80\n"
"v_mfma_f32_16x16x32_bf16 v[160:163], a[208:211], a[168:171], v[160:163]\n"
"v_exp_f32 v84, -v84\n"
"v_mfma_f32_16x16x32_bf16 v[176:179], a[208:211], a[172:175], v[176:179]\n"
"ds_read_b128 a[136:139], v19 offset:4096\n"
"v_fmac_f32 v92, v92, v84\n"
"v_mfma_f32_16x16x32_bf16 v[192:195], a[208:211], a[176:179], v[192:195]\n"
"ds_read_b128 a[140:143], v19 offset:6144\n"
"v_accvgpr_read_b32 v68, a88\n"
"v_mfma_f32_16x16x32_bf16 v[208:211], a[208:211], a[180:183], v[208:211]\n"
"s_waitcnt lgkmcnt(4)\n"
"v_mul_f32 v64, s68, v64\n"
"v_mfma_f32_16x16x32_bf16 v[224:227], a[208:211], a[184:187], v[224:227]\n"
"buffer_load_dwordx4 v29, s[44:47], s61 offen offset:0 lds\n"
"v_mul_f32 v68, s69, v68\n"
"v_mfma_f32_16x16x32_bf16 v[240:243], a[208:211], a[188:191], v[240:243]\n"
"ds_read_b128 a[144:147], v19 offset:8192\n"
"v_accvgpr_read_b32 v65, a81\n"
"v_mfma_f32_16x16x32_bf16 v[132:135], a[212:215], a[160:163], v[132:135]\n"
"v_rcp_f32 v92, v92\n"
"v_mfma_f32_16x16x32_bf16 v[148:151], a[212:215], a[164:167], v[148:151]\n"
"buffer_load_dwordx4 v30, s[44:47], s61 offen offset:1024 lds\n"
"v_fma_f32 v88, -v88, v92, v92\n"
"v_mfma_f32_16x16x32_bf16 v[164:167], a[212:215], a[168:171], v[164:167]\n"
"ds_read_b128 a[148:151], v19 offset:10240\n"
"v_bfi_b32 v88, s70, v88, v80\n"
"v_mfma_f32_16x16x32_bf16 v[180:183], a[212:215], a[172:175], v[180:183]\n"
"v_exp_f32 v89, -|v81|\n"
"v_mfma_f32_16x16x32_bf16 v[196:199], a[212:215], a[176:179], v[196:199]\n"
"buffer_load_dwordx4 v31, s[44:47], s61 offen offset:2048 lds\n"
"v_add_f32 v93, 1.0, v89\n"
"v_mfma_f32_16x16x32_bf16 v[212:215], a[212:215], a[180:183], v[212:215]\n"
"ds_read_b128 a[152:155], v19 offset:12288\n"
"v_accvgpr_read_b32 v69, a89\n"
"v_mfma_f32_16x16x32_bf16 v[228:231], a[212:215], a[184:187], v[228:231]\n"
"v_exp_f32 v85, -v85\n"
"v_mfma_f32_16x16x32_bf16 v[244:247], a[212:215], a[188:191], v[244:247]\n"
"buffer_load_dwordx4 v32, s[44:47], s61 offen offset:3072 lds\n"
"v_fmac_f32 v93, v93, v85\n"
"v_mfma_f32_16x16x32_bf16 v[136:139], a[216:219], a[160:163], v[136:139]\n"
"ds_read_b128 a[156:159], v19 offset:14336\n"
"v_mul_f32 v65, s68, v65\n"
"v_mfma_f32_16x16x32_bf16 v[152:155], a[216:219], a[164:167], v[152:155]\n"
"s_waitcnt vmcnt(20)\n"
"v_mul_f32 v69, s69, v69\n"
"v_mfma_f32_16x16x32_bf16 v[168:171], a[216:219], a[168:171], v[168:171]\n"
"ds_read_b64_tr_b16 a[192:193], v21 offset:4096\n"
"v_accvgpr_read_b32 v66, a82\n"
"v_mfma_f32_16x16x32_bf16 v[184:187], a[216:219], a[172:175], v[184:187]\n"
"ds_read_b64_tr_b16 a[194:195], v25 offset:4096\n"
"v_accvgpr_read_b32 v70, a90\n"
"v_mfma_f32_16x16x32_bf16 v[200:203], a[216:219], a[176:179], v[200:203]\n"
"v_rcp_f32 v93, v93\n"
"v_mfma_f32_16x16x32_bf16 v[216:219], a[216:219], a[180:183], v[216:219]\n"
"ds_read_b64_tr_b16 a[196:197], v22 offset:4096\n"
"v_fma_f32 v89, -v89, v93, v93\n"
"v_mfma_f32_16x16x32_bf16 v[232:235], a[216:219], a[184:187], v[232:235]\n"
"ds_read_b64_tr_b16 a[198:199], v26 offset:4096\n"
"v_bfi_b32 v89, s70, v89, v81\n"
"v_mfma_f32_16x16x32_bf16 v[248:251], a[216:219], a[188:191], v[248:251]\n"
"v_exp_f32 v90, -|v82|\n"
"v_mfma_f32_16x16x32_bf16 v[140:143], a[220:223], a[160:163], v[140:143]\n"
"ds_read_b64_tr_b16 a[200:201], v23 offset:4096\n"
"v_add_f32 v94, 1.0, v90\n"
"v_mfma_f32_16x16x32_bf16 v[156:159], a[220:223], a[164:167], v[156:159]\n"
"ds_read_b64_tr_b16 a[202:203], v27 offset:4096\n"
"v_cvt_pk_bf16_f32 v112, v88, v89\n"
"v_mfma_f32_16x16x32_bf16 v[172:175], a[220:223], a[168:171], v[172:175]\n"
"v_exp_f32 v86, -v86\n"
"v_mfma_f32_16x16x32_bf16 v[188:191], a[220:223], a[172:175], v[188:191]\n"
"ds_read_b64_tr_b16 a[204:205], v24 offset:4096\n"
"v_fmac_f32 v94, v94, v86\n"
"v_mfma_f32_16x16x32_bf16 v[204:207], a[220:223], a[176:179], v[204:207]\n"
"ds_read_b64_tr_b16 a[206:207], v28 offset:4096\n"
"v_mul_f32 v66, s68, v66\n"
"v_mfma_f32_16x16x32_bf16 v[220:223], a[220:223], a[180:183], v[220:223]\n"
"v_rcp_f32 v94, v94\n"
"v_mfma_f32_16x16x32_bf16 v[236:239], a[220:223], a[184:187], v[236:239]\n"
"s_add_u32 m0, s62, 4096\n"
"s_add_u32 s61, s61, 0x8000\n"
"v_mfma_f32_16x16x32_bf16 v[252:255], a[220:223], a[188:191], v[252:255]\n"
"s_waitcnt lgkmcnt(4)\n"
"v_fma_f32 v90, -v90, v94, v94\n"
"v_mfma_f32_16x16x32_bf16 v[128:131], a[192:195], a[128:131], v[128:131]\n"
"ds_read_b128 a[160:163], v19 offset:512\n"
"v_bfi_b32 v90, s70, v90, v82\n"
"v_mfma_f32_16x16x32_bf16 v[144:147], a[192:195], a[132:135], v[144:147]\n"
"ds_read_b128 a[164:167], v19 offset:2560\n"
"v_mul_f32 v70, s69, v70\n"
"v_mfma_f32_16x16x32_bf16 v[160:163], a[192:195], a[136:139], v[160:163]\n"
"v_exp_f32 v91, -|v83|\n"
"v_mfma_f32_16x16x32_bf16 v[176:179], a[192:195], a[140:143], v[176:179]\n"
"ds_read_b128 a[168:171], v19 offset:4608\n"
"v_add_f32 v95, 1.0, v91\n"
"v_mfma_f32_16x16x32_bf16 v[192:195], a[192:195], a[144:147], v[192:195]\n"
"ds_read_b128 a[172:175], v19 offset:6656\n"
"v_accvgpr_read_b32 v67, a83\n"
"v_mfma_f32_16x16x32_bf16 v[208:211], a[192:195], a[148:151], v[208:211]\n"
"s_waitcnt lgkmcnt(4)\n"
"v_accvgpr_read_b32 v71, a91\n"
"v_mfma_f32_16x16x32_bf16 v[224:227], a[192:195], a[152:155], v[224:227]\n"
"buffer_load_dwordx4 v29, s[44:47], s61 offen offset:0 lds\n"
"v_mul_f32 v67, s68, v67\n"
"v_mfma_f32_16x16x32_bf16 v[240:243], a[192:195], a[156:159], v[240:243]\n"
"ds_read_b128 a[176:179], v19 offset:8704\n"
"v_mul_f32 v71, s69, v71\n"
"v_mfma_f32_16x16x32_bf16 v[132:135], a[196:199], a[128:131], v[132:135]\n"
"v_exp_f32 v87, -v87\n"
"v_mfma_f32_16x16x32_bf16 v[148:151], a[196:199], a[132:135], v[148:151]\n"
"buffer_load_dwordx4 v30, s[44:47], s61 offen offset:1024 lds\n"
"v_fmac_f32 v95, v95, v87\n"
"v_mfma_f32_16x16x32_bf16 v[164:167], a[196:199], a[136:139], v[164:167]\n"
"ds_read_b128 a[180:183], v19 offset:10752\n"
"v_mfma_f32_16x16x32_bf16 v[180:183], a[196:199], a[140:143], v[180:183]\n"
"v_rcp_f32 v95, v95\n"
"v_mfma_f32_16x16x32_bf16 v[196:199], a[196:199], a[144:147], v[196:199]\n"
"buffer_load_dwordx4 v31, s[44:47], s61 offen offset:2048 lds\n"
"v_fma_f32 v91, -v91, v95, v95\n"
"v_mfma_f32_16x16x32_bf16 v[212:215], a[196:199], a[148:151], v[212:215]\n"
"ds_read_b128 a[184:187], v19 offset:12800\n"
"v_bfi_b32 v91, s70, v91, v83\n"
"v_mfma_f32_16x16x32_bf16 v[228:231], a[196:199], a[152:155], v[228:231]\n"
"v_exp_f32 v56, -|v48|\n"
"v_mfma_f32_16x16x32_bf16 v[244:247], a[196:199], a[156:159], v[244:247]\n"
"buffer_load_dwordx4 v32, s[44:47], s61 offen offset:3072 lds\n"
"v_cvt_pk_bf16_f32 v113, v90, v91\n"
"v_mfma_f32_16x16x32_bf16 v[136:139], a[200:203], a[128:131], v[136:139]\n"
"ds_read_b128 a[188:191], v19 offset:14848\n"
"v_add_f32 v60, 1.0, v56\n"
"v_mfma_f32_16x16x32_bf16 v[152:155], a[200:203], a[132:135], v[152:155]\n"
"s_waitcnt vmcnt(12)\n"
"v_accvgpr_read_b32 v80, a84\n"
"v_mfma_f32_16x16x32_bf16 v[168:171], a[200:203], a[136:139], v[168:171]\n"
"ds_read_b64_tr_b16 a[208:209], v21 offset:8192\n"
"v_add_u32 v19, 17408, v19\n"
"v_mfma_f32_16x16x32_bf16 v[184:187], a[200:203], a[140:143], v[184:187]\n"
"ds_read_b64_tr_b16 a[210:211], v25 offset:8192\n"
"v_accvgpr_read_b32 v84, a92\n"
"v_mfma_f32_16x16x32_bf16 v[200:203], a[200:203], a[144:147], v[200:203]\n"
"v_exp_f32 v52, -v52\n"
"v_mfma_f32_16x16x32_bf16 v[216:219], a[200:203], a[148:151], v[216:219]\n"
"ds_read_b64_tr_b16 a[212:213], v22 offset:8192\n"
"v_fmac_f32 v60, v60, v52\n"
"v_mfma_f32_16x16x32_bf16 v[232:235], a[200:203], a[152:155], v[232:235]\n"
"ds_read_b64_tr_b16 a[214:215], v26 offset:8192\n"
"v_mul_f32 v80, s68, v80\n"
"v_mfma_f32_16x16x32_bf16 v[248:251], a[200:203], a[156:159], v[248:251]\n"
"v_rcp_f32 v60, v60\n"
"v_mfma_f32_16x16x32_bf16 v[140:143], a[204:207], a[128:131], v[140:143]\n"
"ds_read_b64_tr_b16 a[216:217], v23 offset:8192\n"
"v_fma_f32 v56, -v56, v60, v60\n"
"v_mfma_f32_16x16x32_bf16 v[156:159], a[204:207], a[132:135], v[156:159]\n"
"ds_read_b64_tr_b16 a[218:219], v27 offset:8192\n"
"v_bfi_b32 v56, s70, v56, v48\n"
"v_mfma_f32_16x16x32_bf16 v[172:175], a[204:207], a[136:139], v[172:175]\n"
"v_exp_f32 v57, -|v49|\n"
"v_mfma_f32_16x16x32_bf16 v[188:191], a[204:207], a[140:143], v[188:191]\n"
"ds_read_b64_tr_b16 a[220:221], v24 offset:8192\n"
"v_add_f32 v61, 1.0, v57\n"
"v_mfma_f32_16x16x32_bf16 v[204:207], a[204:207], a[144:147], v[204:207]\n"
"ds_read_b64_tr_b16 a[222:223], v28 offset:8192\n"
"v_mul_f32 v84, s69, v84\n"
"v_mfma_f32_16x16x32_bf16 v[220:223], a[204:207], a[148:151], v[220:223]\n"
"v_exp_f32 v53, -v53\n"
"v_mfma_f32_16x16x32_bf16 v[236:239], a[204:207], a[152:155], v[236:239]\n"
"s_add_u32 m0, s62, 8192\n"
"s_add_u32 s61, s49, s53\n"
"v_mfma_f32_16x16x32_bf16 v[252:255], a[204:207], a[156:159], v[252:255]\n"
"s_waitcnt lgkmcnt(4)\n"
"v_fmac_f32 v61, v61, v53\n"
"v_mfma_f32_16x16x32_bf16 v[128:131], a[208:211], a[160:163], v[128:131]\n"
"ds_read_b128 a[128:131], v20 offset:0\n"
"v_accvgpr_read_b32 v81, a85\n"
"v_mfma_f32_16x16x32_bf16 v[144:147], a[208:211], a[164:167], v[144:147]\n"
"ds_read_b128 a[132:135], v20 offset:2048\n"
"v_accvgpr_read_b32 v85, a93\n"
"v_mfma_f32_16x16x32_bf16 v[160:163], a[208:211], a[168:171], v[160:163]\n"
"ds_read_b64_tr_b16 a[16:17], v37 offset:0\n"
"ds_read_b64_tr_b16 a[18:19], v39 offset:0\n"
"v_mfma_f32_16x16x32_bf16 v[176:179], a[208:211], a[172:175], v[176:179]\n"
"ds_read_b128 a[136:139], v20 offset:4096\n"
"v_mul_f32 v81, s68, v81\n"
"v_mfma_f32_16x16x32_bf16 v[192:195], a[208:211], a[176:179], v[192:195]\n"
"ds_read_b128 a[140:143], v20 offset:6144\n"
"v_mul_f32 v85, s69, v85\n"
"v_mfma_f32_16x16x32_bf16 v[208:211], a[208:211], a[180:183], v[208:211]\n"
"s_waitcnt lgkmcnt(6)\n"
"v_accvgpr_read_b32 v82, a86\n"
"v_mfma_f32_16x16x32_bf16 v[224:227], a[208:211], a[184:187], v[224:227]\n"
"buffer_load_dwordx4 v29, s[44:47], s61 offen offset:0 lds\n"
"v_accvgpr_read_b32 v86, a94\n"
"v_mfma_f32_16x16x32_bf16 v[240:243], a[208:211], a[188:191], v[240:243]\n"
"ds_read_b128 a[144:147], v20 offset:8192\n"
"v_mul_f32 v82, s68, v82\n"
"v_mfma_f32_16x16x32_bf16 v[132:135], a[212:215], a[160:163], v[132:135]\n"
"ds_read_b64_tr_b16 a[20:21], v38 offset:0\n"
"ds_read_b64_tr_b16 a[22:23], v40 offset:0\n"
"v_mfma_f32_16x16x32_bf16 v[148:151], a[212:215], a[164:167], v[148:151]\n"
"buffer_load_dwordx4 v30, s[44:47], s61 offen offset:1024 lds\n"
"v_mul_f32 v86, s69, v86\n"
"v_mfma_f32_16x16x32_bf16 v[164:167], a[212:215], a[168:171], v[164:167]\n"
"ds_read_b128 a[148:151], v20 offset:10240\n"
"v_accvgpr_read_b32 v83, a87\n"
"v_mfma_f32_16x16x32_bf16 v[180:183], a[212:215], a[172:175], v[180:183]\n"
"ds_read_b64_tr_b16 a[24:25], v37 offset:512\n"
"ds_read_b64_tr_b16 a[26:27], v39 offset:512\n"
"v_mfma_f32_16x16x32_bf16 v[196:199], a[212:215], a[176:179], v[196:199]\n"
"buffer_load_dwordx4 v31, s[44:47], s61 offen offset:2048 lds\n"
"v_accvgpr_read_b32 v87, a95\n"
"v_mfma_f32_16x16x32_bf16 v[212:215], a[212:215], a[180:183], v[212:215]\n"
"ds_read_b128 a[152:155], v20 offset:12288\n"
"v_mul_f32 v83, s68, v83\n"
"v_mfma_f32_16x16x32_bf16 v[228:231], a[212:215], a[184:187], v[228:231]\n"
"ds_read_b64_tr_b16 a[28:29], v38 offset:512\n"
"ds_read_b64_tr_b16 a[30:31], v40 offset:512\n"
"v_mfma_f32_16x16x32_bf16 v[244:247], a[212:215], a[188:191], v[244:247]\n"
"buffer_load_dwordx4 v32, s[44:47], s61 offen offset:3072 lds\n"
"s_xor_b32 s99, s98, 0\n"
"v_mfma_f32_16x16x32_bf16 v[136:139], a[216:219], a[160:163], v[136:139]\n"
"ds_read_b128 a[156:159], v20 offset:14336\n"
"s_add_u32 s99, s99, 32768\n"
"v_mfma_f32_16x16x32_bf16 v[152:155], a[216:219], a[164:167], v[152:155]\n"
"s_waitcnt vmcnt(20)\n"
"v_mul_f32 v87, s69, v87\n"
"v_mfma_f32_16x16x32_bf16 v[168:171], a[216:219], a[168:171], v[168:171]\n"
"ds_read_b64_tr_b16 a[192:193], v21 offset:12288\n"
"s_lshl_b32 m0, %[sm0], 2\n"
"v_mfma_f32_16x16x32_bf16 v[184:187], a[216:219], a[172:175], v[184:187]\n"
"ds_read_b64_tr_b16 a[194:195], v25 offset:12288\n"
"s_waitcnt lgkmcnt(3)\n"
"v_mfma_f32_16x16x32_bf16 v[200:203], a[216:219], a[176:179], v[200:203]\n"
"buffer_load_dwordx4 v33, s[64:67], s99 offen offset:0 lds\n"
"v_mfma_f32_16x16x32_bf16 v[216:219], a[216:219], a[180:183], v[216:219]\n"
"ds_read_b64_tr_b16 a[196:197], v22 offset:12288\n"
"v_mfma_f32_16x16x32_bf16 v[232:235], a[216:219], a[184:187], v[232:235]\n"
"ds_read_b64_tr_b16 a[198:199], v26 offset:12288\n"
"v_mfma_f32_16x16x32_bf16 v[248:251], a[216:219], a[188:191], v[248:251]\n"
"buffer_load_dwordx4 v34, s[64:67], s99 offen offset:1024 lds\n"
"v_mfma_f32_16x16x32_bf16 v[140:143], a[220:223], a[160:163], v[140:143]\n"
"ds_read_b64_tr_b16 a[200:201], v23 offset:12288\n"
"v_mfma_f32_16x16x32_bf16 v[156:159], a[220:223], a[164:167], v[156:159]\n"
"ds_read_b64_tr_b16 a[202:203], v27 offset:12288\n"
"v_mfma_f32_16x16x32_bf16 v[172:175], a[220:223], a[168:171], v[172:175]\n"
"buffer_load_dwordx4 v35, s[64:67], s99 offen offset:2048 lds\n"
"v_mfma_f32_16x16x32_bf16 v[188:191], a[220:223], a[172:175], v[188:191]\n"
"ds_read_b64_tr_b16 a[204:205], v24 offset:12288\n"
"v_mfma_f32_16x16x32_bf16 v[204:207], a[220:223], a[176:179], v[204:207]\n"
"ds_read_b64_tr_b16 a[206:207], v28 offset:12288\n"
"v_mfma_f32_16x16x32_bf16 v[220:223], a[220:223], a[180:183], v[220:223]\n"
"buffer_load_dwordx4 v36, s[64:67], s99 offen offset:3072 lds\n"
"v_mfma_f32_16x16x32_bf16 v[236:239], a[220:223], a[184:187], v[236:239]\n"
"s_add_u32 m0, s62, 12288\n"
"s_add_u32 s61, s61, 0x8000\n"
"v_mfma_f32_16x16x32_bf16 v[252:255], a[220:223], a[188:191], v[252:255]\n"
"s_waitcnt lgkmcnt(4)\n"
"v_mfma_f32_16x16x32_bf16 v[128:131], a[192:195], a[128:131], v[128:131]\n"
"ds_read_b128 a[160:163], v20 offset:512\n"
"v_mfma_f32_16x16x32_bf16 v[144:147], a[192:195], a[132:135], v[144:147]\n"
"ds_read_b128 a[164:167], v20 offset:2560\n"
"v_mfma_f32_16x16x32_bf16 v[160:163], a[192:195], a[136:139], v[160:163]\n"
"v_rcp_f32 v61, v61\n"
"v_mfma_f32_16x16x32_bf16 v[176:179], a[192:195], a[140:143], v[176:179]\n"
"ds_read_b128 a[168:171], v20 offset:4608\n"
"v_fma_f32 v57, -v57, v61, v61\n"
"v_mfma_f32_16x16x32_bf16 v[192:195], a[192:195], a[144:147], v[192:195]\n"
"ds_read_b128 a[172:175], v20 offset:6656\n"
"v_bfi_b32 v57, s70, v57, v49\n"
"v_mfma_f32_16x16x32_bf16 v[208:211], a[192:195], a[148:151], v[208:211]\n"
"s_waitcnt lgkmcnt(4)\n"
"v_cvt_pk_bf16_f32 v114, v56, v57\n"
"v_mfma_f32_16x16x32_bf16 v[224:227], a[192:195], a[152:155], v[224:227]\n"
"buffer_load_dwordx4 v29, s[44:47], s61 offen offset:0 lds\n"
"v_mfma_f32_16x16x32_bf16 v[240:243], a[192:195], a[156:159], v[240:243]\n"
"ds_read_b128 a[176:179], v20 offset:8704\n"
"v_mfma_f32_16x16x32_bf16 v[132:135], a[196:199], a[128:131], v[132:135]\n"
"v_exp_f32 v58, -|v50|\n"
"v_mfma_f32_16x16x32_bf16 v[148:151], a[196:199], a[132:135], v[148:151]\n"
"buffer_load_dwordx4 v30, s[44:47], s61 offen offset:1024 lds\n"
"v_add_f32 v62, 1.0, v58\n"
"v_mfma_f32_16x16x32_bf16 v[164:167], a[196:199], a[136:139], v[164:167]\n"
"ds_read_b128 a[180:183], v20 offset:10752\n"
"v_mfma_f32_16x16x32_bf16 v[180:183], a[196:199], a[140:143], v[180:183]\n"
"v_exp_f32 v54, -v54\n"
"v_mfma_f32_16x16x32_bf16 v[196:199], a[196:199], a[144:147], v[196:199]\n"
"buffer_load_dwordx4 v31, s[44:47], s61 offen offset:2048 lds\n"
"v_fmac_f32 v62, v62, v54\n"
"v_mfma_f32_16x16x32_bf16 v[212:215], a[196:199], a[148:151], v[212:215]\n"
"ds_read_b128 a[184:187], v20 offset:12800\n"
"v_mfma_f32_16x16x32_bf16 v[228:231], a[196:199], a[152:155], v[228:231]\n"
"v_rcp_f32 v62, v62\n"
"v_mfma_f32_16x16x32_bf16 v[244:247], a[196:199], a[156:159], v[244:247]\n"
"buffer_load_dwordx4 v32, s[44:47], s61 offen offset:3072 lds\n"
"v_fma_f32 v58, -v58, v62, v62\n"
"v_mfma_f32_16x16x32_bf16 v[136:139], a[200:203], a[128:131], v[136:139]\n"
"ds_read_b128 a[188:191], v20 offset:14848\n"
"v_bfi_b32 v58, s70, v58, v50\n"
"v_mfma_f32_16x16x32_bf16 v[152:155], a[200:203], a[132:135], v[152:155]\n"
"s_waitcnt vmcnt(20)\n"
"v_mfma_f32_16x16x32_bf16 v[168:171], a[200:203], a[136:139], v[168:171]\n"
"ds_read_b64_tr_b16 a[208:209], v21 offset:16384\n"
"v_add_u32 v20, 17408, v20\n"
"v_mfma_f32_16x16x32_bf16 v[184:187], a[200:203], a[140:143], v[184:187]\n"
"ds_read_b64_tr_b16 a[210:211], v25 offset:16384\n"
"v_mfma_f32_16x16x32_bf16 v[200:203], a[200:203], a[144:147], v[200:203]\n"
"v_exp_f32 v59, -|v51|\n"
"v_mfma_f32_16x16x32_bf16 v[216:219], a[200:203], a[148:151], v[216:219]\n"
"ds_read_b64_tr_b16 a[212:213], v22 offset:16384\n"
"v_add_f32 v63, 1.0, v59\n"
"v_mfma_f32_16x16x32_bf16 v[232:235], a[200:203], a[152:155], v[232:235]\n"
"ds_read_b64_tr_b16 a[214:215], v26 offset:16384\n"
"v_mfma_f32_16x16x32_bf16 v[248:251], a[200:203], a[156:159], v[248:251]\n"
"v_exp_f32 v55, -v55\n"
"v_mfma_f32_16x16x32_bf16 v[140:143], a[204:207], a[128:131], v[140:143]\n"
"ds_read_b64_tr_b16 a[216:217], v23 offset:16384\n"
"v_fmac_f32 v63, v63, v55\n"
"v_mfma_f32_16x16x32_bf16 v[156:159], a[204:207], a[132:135], v[156:159]\n"
"ds_read_b64_tr_b16 a[218:219], v27 offset:16384\n"
"v_mfma_f32_16x16x32_bf16 v[172:175], a[204:207], a[136:139], v[172:175]\n"
"v_rcp_f32 v63, v63\n"
"v_mfma_f32_16x16x32_bf16 v[188:191], a[204:207], a[140:143], v[188:191]\n"
"ds_read_b64_tr_b16 a[220:221], v24 offset:16384\n"
"v_fma_f32 v59, -v59, v63, v63\n"
"v_mfma_f32_16x16x32_bf16 v[204:207], a[204:207], a[144:147], v[204:207]\n"
"ds_read_b64_tr_b16 a[222:223], v28 offset:16384\n"
"v_bfi_b32 v59, s70, v59, v51\n"
"v_mfma_f32_16x16x32_bf16 v[220:223], a[204:207], a[148:151], v[220:223]\n"
"v_exp_f32 v72, -|v64|\n"
"v_mfma_f32_16x16x32_bf16 v[236:239], a[204:207], a[152:155], v[236:239]\n"
"s_add_u32 m0, s62, 16384\n"
"s_add_u32 s61, s50, s53\n"
"v_mfma_f32_16x16x32_bf16 v[252:255], a[204:207], a[156:159], v[252:255]\n"
"s_waitcnt lgkmcnt(4)\n"
"v_cvt_pk_bf16_f32 v115, v58, v59\n"
"v_mfma_f32_16x16x32_bf16 v[128:131], a[208:211], a[160:163], v[128:131]\n"
"ds_read_b128 a[128:131], v16 offset:0\n"
"v_add_f32 v76, 1.0, v72\n"
"v_mfma_f32_16x16x32_bf16 v[144:147], a[208:211], a[164:167], v[144:147]\n"
"ds_read_b128 a[132:135], v16 offset:2048\n"
"v_accvgpr_read_b32 v48, a96\n"
"v_mfma_f32_16x16x32_bf16 v[160:163], a[208:211], a[168:171], v[160:163]\n"
"v_exp_f32 v68, -v68\n"
"v_mfma_f32_16x16x32_bf16 v[176:179], a[208:211], a[172:175], v[176:179]\n"
"ds_read_b128 a[136:139], v16 offset:4096\n"
"v_fmac_f32 v76, v76, v68\n"
"v_mfma_f32_16x16x32_bf16 v[192:195], a[208:211], a[176:179], v[192:195]\n"
"ds_read_b128 a[140:143], v16 offset:6144\n"
"v_accvgpr_read_b32 v52, a104\n"
"v_mfma_f32_16x16x32_bf16 v[208:211], a[208:211], a[180:183], v[208:211]\n"
"s_waitcnt lgkmcnt(4)\n"
"v_mul_f32 v48, s68, v48\n"
"v_mfma_f32_16x16x32_bf16 v[224:227], a[208:211], a[184:187], v[224:227]\n"
"buffer_load_dwordx4 v29, s[44:47], s61 offen offset:0 lds\n"
"v_mul_f32 v52, s69, v52\n"
"v_mfma_f32_16x16x32_bf16 v[240:243], a[208:211], a[188:191], v[240:243]\n"
"ds_read_b128 a[144:147], v16 offset:8192\n"
"v_accvgpr_read_b32 v49, a97\n"
"v_mfma_f32_16x16x32_bf16 v[132:135], a[212:215], a[160:163], v[132:135]\n"
"v_rcp_f32 v76, v76\n"
"v_mfma_f32_16x16x32_bf16 v[148:151], a[212:215], a[164:167], v[148:151]\n"
"buffer_load_dwordx4 v30, s[44:47], s61 offen offset:1024 lds\n"
"v_fma_f32 v72, -v72, v76, v76\n"
"v_mfma_f32_16x16x32_bf16 v[164:167], a[212:215], a[168:171], v[164:167]\n"
"ds_read_b128 a[148:151], v16 offset:10240\n"
"v_bfi_b32 v72, s70, v72, v64\n"
"v_mfma_f32_16x16x32_bf16 v[180:183], a[212:215], a[172:175], v[180:183]\n"
"v_exp_f32 v73, -|v65|\n"
"v_mfma_f32_16x16x32_bf16 v[196:199], a[212:215], a[176:179], v[196:199]\n"
"buffer_load_dwordx4 v31, s[44:47], s61 offen offset:2048 lds\n"
"v_add_f32 v77, 1.0, v73\n"
"v_mfma_f32_16x16x32_bf16 v[212:215], a[212:215], a[180:183], v[212:215]\n"
"ds_read_b128 a[152:155], v16 offset:12288\n"
"v_accvgpr_read_b32 v53, a105\n"
"v_mfma_f32_16x16x32_bf16 v[228:231], a[212:215], a[184:187], v[228:231]\n"
"v_exp_f32 v69, -v69\n"
"v_mfma_f32_16x16x32_bf16 v[244:247], a[212:215], a[188:191], v[244:247]\n"
"buffer_load_dwordx4 v32, s[44:47], s61 offen offset:3072 lds\n"
"v_fmac_f32 v77, v77, v69\n"
"v_mfma_f32_16x16x32_bf16 v[136:139], a[216:219], a[160:163], v[136:139]\n"
"ds_read_b128 a[156:159], v16 offset:14336\n"
"v_mul_f32 v49, s68, v49\n"
"v_mfma_f32_16x16x32_bf16 v[152:155], a[216:219], a[164:167], v[152:155]\n"
"s_waitcnt vmcnt(20)\n"
"v_mul_f32 v53, s69, v53\n"
"v_mfma_f32_16x16x32_bf16 v[168:171], a[216:219], a[168:171], v[168:171]\n"
"ds_read_b64_tr_b16 a[192:193], v21 offset:0\n"
"v_accvgpr_read_b32 v50, a98\n"
"v_mfma_f32_16x16x32_bf16 v[184:187], a[216:219], a[172:175], v[184:187]\n"
"ds_read_b64_tr_b16 a[194:195], v25 offset:0\n"
"v_accvgpr_read_b32 v54, a106\n"
"v_mfma_f32_16x16x32_bf16 v[200:203], a[216:219], a[176:179], v[200:203]\n"
"v_rcp_f32 v77, v77\n"
"v_mfma_f32_16x16x32_bf16 v[216:219], a[216:219], a[180:183], v[216:219]\n"
"ds_read_b64_tr_b16 a[196:197], v22 offset:0\n"
"v_fma_f32 v73, -v73, v77, v77\n"
"v_mfma_f32_16x16x32_bf16 v[232:235], a[216:219], a[184:187], v[232:235]\n"
"ds_read_b64_tr_b16 a[198:199], v26 offset:0\n"
"v_bfi_b32 v73, s70, v73, v65\n"
"v_mfma_f32_16x16x32_bf16 v[248:251], a[216:219], a[188:191], v[248:251]\n"
"v_exp_f32 v74, -|v66|\n"
"v_mfma_f32_16x16x32_bf16 v[140:143], a[220:223], a[160:163], v[140:143]\n"
"ds_read_b64_tr_b16 a[200:201], v23 offset:0\n"
"v_add_f32 v78, 1.0, v74\n"
"v_mfma_f32_16x16x32_bf16 v[156:159], a[220:223], a[164:167], v[156:159]\n"
"ds_read_b64_tr_b16 a[202:203], v27 offset:0\n"
"v_cvt_pk_bf16_f32 v116, v72, v73\n"
"v_mfma_f32_16x16x32_bf16 v[172:175], a[220:223], a[168:171], v[172:175]\n"
"v_exp_f32 v70, -v70\n"
"v_mfma_f32_16x16x32_bf16 v[188:191], a[220:223], a[172:175], v[188:191]\n"
"ds_read_b64_tr_b16 a[204:205], v24 offset:0\n"
"v_fmac_f32 v78, v78, v70\n"
"v_mfma_f32_16x16x32_bf16 v[204:207], a[220:223], a[176:179], v[204:207]\n"
"ds_read_b64_tr_b16 a[206:207], v28 offset:0\n"
"v_mul_f32 v50, s68, v50\n"
"v_mfma_f32_16x16x32_bf16 v[220:223], a[220:223], a[180:183], v[220:223]\n"
"v_rcp_f32 v78, v78\n"
"v_mfma_f32_16x16x32_bf16 v[236:239], a[220:223], a[184:187], v[236:239]\n"
"s_add_u32 m0, s62, 0\n"
"s_add_u32 s61, s61, 0x8000\n"
"v_mfma_f32_16x16x32_bf16 v[252:255], a[220:223], a[188:191], v[252:255]\n"
"s_waitcnt lgkmcnt(4)\n"
"v_fma_f32 v74, -v74, v78, v78\n"
"v_mfma_f32_16x16x32_bf16 v[128:131], a[192:195], a[128:131], v[128:131]\n"
"ds_read_b128 a[160:163], v16 offset:512\n"
"v_bfi_b32 v74, s70, v74, v66\n"
"v_mfma_f32_16x16x32_bf16 v[144:147], a[192:195], a[132:135], v[144:147]\n"
"ds_read_b128 a[164:167], v16 offset:2560\n"
"v_mul_f32 v54, s69, v54\n"
"v_mfma_f32_16x16x32_bf16 v[160:163], a[192:195], a[136:139], v[160:163]\n"
"v_exp_f32 v75, -|v67|\n"
"v_mfma_f32_16x16x32_bf16 v[176:179], a[192:195], a[140:143], v[176:179]\n"
"ds_read_b128 a[168:171], v16 offset:4608\n"
"v_add_f32 v79, 1.0, v75\n"
"v_mfma_f32_16x16x32_bf16 v[192:195], a[192:195], a[144:147], v[192:195]\n"
"ds_read_b128 a[172:175], v16 offset:6656\n"
"v_accvgpr_read_b32 v51, a99\n"
"v_mfma_f32_16x16x32_bf16 v[208:211], a[192:195], a[148:151], v[208:211]\n"
"s_waitcnt lgkmcnt(4)\n"
"v_accvgpr_read_b32 v55, a107\n"
"v_mfma_f32_16x16x32_bf16 v[224:227], a[192:195], a[152:155], v[224:227]\n"
"buffer_load_dwordx4 v29, s[44:47], s61 offen offset:0 lds\n"
"v_mul_f32 v51, s68, v51\n"
"v_mfma_f32_16x16x32_bf16 v[240:243], a[192:195], a[156:159], v[240:243]\n"
"ds_read_b128 a[176:179], v16 offset:8704\n"
"v_mul_f32 v55, s69, v55\n"
"v_mfma_f32_16x16x32_bf16 v[132:135], a[196:199], a[128:131], v[132:135]\n"
"v_exp_f32 v71, -v71\n"
"v_mfma_f32_16x16x32_bf16 v[148:151], a[196:199], a[132:135], v[148:151]\n"
"buffer_load_dwordx4 v30, s[44:47], s61 offen offset:1024 lds\n"
"v_fmac_f32 v79, v79, v71\n"
"v_mfma_f32_16x16x32_bf16 v[164:167], a[196:199], a[136:139], v[164:167]\n"
"ds_read_b128 a[180:183], v16 offset:10752\n"
"v_mfma_f32_16x16x32_bf16 v[180:183], a[196:199], a[140:143], v[180:183]\n"
"v_rcp_f32 v79, v79\n"
"v_mfma_f32_16x16x32_bf16 v[196:199], a[196:199], a[144:147], v[196:199]\n"
"buffer_load_dwordx4 v31, s[44:47], s61 offen offset:2048 lds\n"
"v_fma_f32 v75, -v75, v79, v79\n"
"v_mfma_f32_16x16x32_bf16 v[212:215], a[196:199], a[148:151], v[212:215]\n"
"ds_read_b128 a[184:187], v16 offset:12800\n"
"v_bfi_b32 v75, s70, v75, v67\n"
"v_mfma_f32_16x16x32_bf16 v[228:231], a[196:199], a[152:155], v[228:231]\n"
"v_exp_f32 v88, -|v80|\n"
"v_mfma_f32_16x16x32_bf16 v[244:247], a[196:199], a[156:159], v[244:247]\n"
"buffer_load_dwordx4 v32, s[44:47], s61 offen offset:3072 lds\n"
"v_cvt_pk_bf16_f32 v117, v74, v75\n"
"v_mfma_f32_16x16x32_bf16 v[136:139], a[200:203], a[128:131], v[136:139]\n"
"ds_read_b128 a[188:191], v16 offset:14848\n"
"v_add_f32 v92, 1.0, v88\n"
"v_mfma_f32_16x16x32_bf16 v[152:155], a[200:203], a[132:135], v[152:155]\n"
"s_waitcnt vmcnt(12)\n"
"v_accvgpr_read_b32 v64, a100\n"
"v_mfma_f32_16x16x32_bf16 v[168:171], a[200:203], a[136:139], v[168:171]\n"
"ds_read_b64_tr_b16 a[208:209], v21 offset:4096\n"
"v_add_u32 v16, 17408, v16\n"
"v_mfma_f32_16x16x32_bf16 v[184:187], a[200:203], a[140:143], v[184:187]\n"
"ds_read_b64_tr_b16 a[210:211], v25 offset:4096\n"
"v_accvgpr_read_b32 v68, a108\n"
"v_mfma_f32_16x16x32_bf16 v[200:203], a[200:203], a[144:147], v[200:203]\n"
"v_exp_f32 v84, -v84\n"
"v_mfma_f32_16x16x32_bf16 v[216:219], a[200:203], a[148:151], v[216:219]\n"
"ds_read_b64_tr_b16 a[212:213], v22 offset:4096\n"
"v_fmac_f32 v92, v92, v84\n"
"v_mfma_f32_16x16x32_bf16 v[232:235], a[200:203], a[152:155], v[232:235]\n"
"ds_read_b64_tr_b16 a[214:215], v26 offset:4096\n"
"v_mul_f32 v64, s68, v64\n"
"v_mfma_f32_16x16x32_bf16 v[248:251], a[200:203], a[156:159], v[248:251]\n"
"v_rcp_f32 v92, v92\n"
"v_mfma_f32_16x16x32_bf16 v[140:143], a[204:207], a[128:131], v[140:143]\n"
"ds_read_b64_tr_b16 a[216:217], v23 offset:4096\n"
"v_fma_f32 v88, -v88, v92, v92\n"
"v_mfma_f32_16x16x32_bf16 v[156:159], a[204:207], a[132:135], v[156:159]\n"
"ds_read_b64_tr_b16 a[218:219], v27 offset:4096\n"
"v_bfi_b32 v88, s70, v88, v80\n"
"v_mfma_f32_16x16x32_bf16 v[172:175], a[204:207], a[136:139], v[172:175]\n"
"v_exp_f32 v89, -|v81|\n"
"v_mfma_f32_16x16x32_bf16 v[188:191], a[204:207], a[140:143], v[188:191]\n"
"ds_read_b64_tr_b16 a[220:221], v24 offset:4096\n"
"v_add_f32 v93, 1.0, v89\n"
"v_mfma_f32_16x16x32_bf16 v[204:207], a[204:207], a[144:147], v[204:207]\n"
"ds_read_b64_tr_b16 a[222:223], v28 offset:4096\n"
"v_mul_f32 v68, s69, v68\n"
"v_mfma_f32_16x16x32_bf16 v[220:223], a[204:207], a[148:151], v[220:223]\n"
"v_exp_f32 v85, -v85\n"
"v_mfma_f32_16x16x32_bf16 v[236:239], a[204:207], a[152:155], v[236:239]\n"
"s_add_u32 m0, s62, 4096\n"
"s_add_u32 s61, s51, s53\n"
"v_mfma_f32_16x16x32_bf16 v[252:255], a[204:207], a[156:159], v[252:255]\n"
"s_waitcnt lgkmcnt(4)\n"
"v_fmac_f32 v93, v93, v85\n"
"v_mfma_f32_16x16x32_bf16 v[128:131], a[208:211], a[160:163], v[128:131]\n"
"ds_read_b128 a[128:131], v17 offset:0\n"
"v_accvgpr_read_b32 v65, a101\n"
"v_mfma_f32_16x16x32_bf16 v[144:147], a[208:211], a[164:167], v[144:147]\n"
"ds_read_b128 a[132:135], v17 offset:2048\n"
"v_accvgpr_read_b32 v69, a109\n"
"v_mfma_f32_16x16x32_bf16 v[160:163], a[208:211], a[168:171], v[160:163]\n"
"ds_read_b64_tr_b16 a[32:33], v37 offset:0\n"
"ds_read_b64_tr_b16 a[34:35], v39 offset:0\n"
"v_mfma_f32_16x16x32_bf16 v[176:179], a[208:211], a[172:175], v[176:179]\n"
"ds_read_b128 a[136:139], v17 offset:4096\n"
"v_mul_f32 v65, s68, v65\n"
"v_mfma_f32_16x16x32_bf16 v[192:195], a[208:211], a[176:179], v[192:195]\n"
"ds_read_b128 a[140:143], v17 offset:6144\n"
"v_mul_f32 v69, s69, v69\n"
"v_mfma_f32_16x16x32_bf16 v[208:211], a[208:211], a[180:183], v[208:211]\n"
"s_waitcnt lgkmcnt(6)\n"
"v_accvgpr_read_b32 v66, a102\n"
"v_mfma_f32_16x16x32_bf16 v[224:227], a[208:211], a[184:187], v[224:227]\n"
"buffer_load_dwordx4 v29, s[44:47], s61 offen offset:0 lds\n"
"v_accvgpr_read_b32 v70, a110\n"
"v_mfma_f32_16x16x32_bf16 v[240:243], a[208:211], a[188:191], v[240:243]\n"
"ds_read_b128 a[144:147], v17 offset:8192\n"
"v_mul_f32 v66, s68, v66\n"
"v_mfma_f32_16x16x32_bf16 v[132:135], a[212:215], a[160:163], v[132:135]\n"
"ds_read_b64_tr_b16 a[36:37], v38 offset:0\n"
"ds_read_b64_tr_b16 a[38:39], v40 offset:0\n"
"v_mfma_f32_16x16x32_bf16 v[148:151], a[212:215], a[164:167], v[148:151]\n"
"buffer_load_dwordx4 v30, s[44:47], s61 offen offset:1024 lds\n"
"v_mul_f32 v70, s69, v70\n"
"v_mfma_f32_16x16x32_bf16 v[164:167], a[212:215], a[168:171], v[164:167]\n"
"ds_read_b128 a[148:151], v17 offset:10240\n"
"v_accvgpr_read_b32 v67, a103\n"
"v_mfma_f32_16x16x32_bf16 v[180:183], a[212:215], a[172:175], v[180:183]\n"
"ds_read_b64_tr_b16 a[40:41], v37 offset:512\n"
"ds_read_b64_tr_b16 a[42:43], v39 offset:512\n"
"v_mfma_f32_16x16x32_bf16 v[196:199], a[212:215], a[176:179], v[196:199]\n"
"buffer_load_dwordx4 v31, s[44:47], s61 offen offset:2048 lds\n"
"v_accvgpr_read_b32 v71, a111\n"
"v_mfma_f32_16x16x32_bf16 v[212:215], a[212:215], a[180:183], v[212:215]\n"
"ds_read_b128 a[152:155], v17 offset:12288\n"
"v_mul_f32 v67, s68, v67\n"
"v_mfma_f32_16x16x32_bf16 v[228:231], a[212:215], a[184:187], v[228:231]\n"
"ds_read_b64_tr_b16 a[44:45], v38 offset:512\n"
"ds_read_b64_tr_b16 a[46:47], v40 offset:512\n"
"v_mfma_f32_16x16x32_bf16 v[244:247], a[212:215], a[188:191], v[244:247]\n"
"buffer_load_dwordx4 v32, s[44:47], s61 offen offset:3072 lds\n"
"s_xor_b32 s99, s98, 0\n"
"v_mfma_f32_16x16x32_bf16 v[136:139], a[216:219], a[160:163], v[136:139]\n"
"ds_read_b128 a[156:159], v17 offset:14336\n"
"s_add_u32 s99, s99, 49152\n"
"v_mfma_f32_16x16x32_bf16 v[152:155], a[216:219], a[164:167], v[152:155]\n"
"s_waitcnt vmcnt(20)\n"
"v_mul_f32 v71, s69, v71\n"
"v_mfma_f32_16x16x32_bf16 v[168:171], a[216:219], a[168:171], v[168:171]\n"
"ds_read_b64_tr_b16 a[192:193], v21 offset:8192\n"
"s_lshl_b32 m0, %[sm0], 2\n"
"v_mfma_f32_16x16x32_bf16 v[184:187], a[216:219], a[172:175], v[184:187]\n"
"ds_read_b64_tr_b16 a[194:195], v25 offset:8192\n"
"s_waitcnt lgkmcnt(3)\n"
"v_mfma_f32_16x16x32_bf16 v[200:203], a[216:219], a[176:179], v[200:203]\n"
"buffer_load_dwordx4 v33, s[64:67], s99 offen offset:0 lds\n"
"v_mfma_f32_16x16x32_bf16 v[216:219], a[216:219], a[180:183], v[216:219]\n"
"ds_read_b64_tr_b16 a[196:197], v22 offset:8192\n"
"v_mfma_f32_16x16x32_bf16 v[232:235], a[216:219], a[184:187], v[232:235]\n"
"ds_read_b64_tr_b16 a[198:199], v26 offset:8192\n"
"v_mfma_f32_16x16x32_bf16 v[248:251], a[216:219], a[188:191], v[248:251]\n"
"buffer_load_dwordx4 v34, s[64:67], s99 offen offset:1024 lds\n"
"v_mfma_f32_16x16x32_bf16 v[140:143], a[220:223], a[160:163], v[140:143]\n"
"ds_read_b64_tr_b16 a[200:201], v23 offset:8192\n"
"v_mfma_f32_16x16x32_bf16 v[156:159], a[220:223], a[164:167], v[156:159]\n"
"ds_read_b64_tr_b16 a[202:203], v27 offset:8192\n"
"v_mfma_f32_16x16x32_bf16 v[172:175], a[220:223], a[168:171], v[172:175]\n"
"buffer_load_dwordx4 v35, s[64:67], s99 offen offset:2048 lds\n"
"v_mfma_f32_16x16x32_bf16 v[188:191], a[220:223], a[172:175], v[188:191]\n"
"ds_read_b64_tr_b16 a[204:205], v24 offset:8192\n"
"v_mfma_f32_16x16x32_bf16 v[204:207], a[220:223], a[176:179], v[204:207]\n"
"ds_read_b64_tr_b16 a[206:207], v28 offset:8192\n"
"v_mfma_f32_16x16x32_bf16 v[220:223], a[220:223], a[180:183], v[220:223]\n"
"buffer_load_dwordx4 v36, s[64:67], s99 offen offset:3072 lds\n"
"v_mfma_f32_16x16x32_bf16 v[236:239], a[220:223], a[184:187], v[236:239]\n"
"s_add_u32 m0, s62, 8192\n"
"s_add_u32 s61, s61, 0x8000\n"
"v_mfma_f32_16x16x32_bf16 v[252:255], a[220:223], a[188:191], v[252:255]\n"
"s_waitcnt lgkmcnt(4)\n"
"v_mfma_f32_16x16x32_bf16 v[128:131], a[192:195], a[128:131], v[128:131]\n"
"ds_read_b128 a[160:163], v17 offset:512\n"
"v_mfma_f32_16x16x32_bf16 v[144:147], a[192:195], a[132:135], v[144:147]\n"
"ds_read_b128 a[164:167], v17 offset:2560\n"
"v_mfma_f32_16x16x32_bf16 v[160:163], a[192:195], a[136:139], v[160:163]\n"
"v_rcp_f32 v93, v93\n"
"v_mfma_f32_16x16x32_bf16 v[176:179], a[192:195], a[140:143], v[176:179]\n"
"ds_read_b128 a[168:171], v17 offset:4608\n"
"v_fma_f32 v89, -v89, v93, v93\n"
"v_mfma_f32_16x16x32_bf16 v[192:195], a[192:195], a[144:147], v[192:195]\n"
"ds_read_b128 a[172:175], v17 offset:6656\n"
"v_bfi_b32 v89, s70, v89, v81\n"
"v_mfma_f32_16x16x32_bf16 v[208:211], a[192:195], a[148:151], v[208:211]\n"
"s_waitcnt lgkmcnt(4)\n"
"v_cvt_pk_bf16_f32 v118, v88, v89\n"
"v_mfma_f32_16x16x32_bf16 v[224:227], a[192:195], a[152:155], v[224:227]\n"
"buffer_load_dwordx4 v29, s[44:47], s61 offen offset:0 lds\n"
"v_mfma_f32_16x16x32_bf16 v[240:243], a[192:195], a[156:159], v[240:243]\n"
"ds_read_b128 a[176:179], v17 offset:8704\n"
"v_mfma_f32_16x16x32_bf16 v[132:135], a[196:199], a[128:131], v[132:135]\n"
"v_exp_f32 v90, -|v82|\n"
"v_mfma_f32_16x16x32_bf16 v[148:151], a[196:199], a[132:135], v[148:151]\n"
"buffer_load_dwordx4 v30, s[44:47], s61 offen offset:1024 lds\n"
"v_add_f32 v94, 1.0, v90\n"
"v_mfma_f32_16x16x32_bf16 v[164:167], a[196:199], a[136:139], v[164:167]\n"
"ds_read_b128 a[180:183], v17 offset:10752\n"
"v_mfma_f32_16x16x32_bf16 v[180:183], a[196:199], a[140:143], v[180:183]\n"
"v_exp_f32 v86, -v86\n"
"v_mfma_f32_16x16x32_bf16 v[196:199], a[196:199], a[144:147], v[196:199]\n"
"buffer_load_dwordx4 v31, s[44:47], s61 offen offset:2048 lds\n"
"v_fmac_f32 v94, v94, v86\n"
"v_mfma_f32_16x16x32_bf16 v[212:215], a[196:199], a[148:151], v[212:215]\n"
"ds_read_b128 a[184:187], v17 offset:12800\n"
"v_mfma_f32_16x16x32_bf16 v[228:231], a[196:199], a[152:155], v[228:231]\n"
"v_rcp_f32 v94, v94\n"
"v_mfma_f32_16x16x32_bf16 v[244:247], a[196:199], a[156:159], v[244:247]\n"
"buffer_load_dwordx4 v32, s[44:47], s61 offen offset:3072 lds\n"
"v_fma_f32 v90, -v90, v94, v94\n"
"v_mfma_f32_16x16x32_bf16 v[136:139], a[200:203], a[128:131], v[136:139]\n"
"ds_read_b128 a[188:191], v17 offset:14848\n"
"v_bfi_b32 v90, s70, v90, v82\n"
"v_mfma_f32_16x16x32_bf16 v[152:155], a[200:203], a[132:135], v[152:155]\n"
"s_waitcnt vmcnt(20)\n"
"v_mfma_f32_16x16x32_bf16 v[168:171], a[200:203], a[136:139], v[168:171]\n"
"ds_read_b64_tr_b16 a[208:209], v21 offset:12288\n"
"v_add_u32 v17, 17408, v17\n"
"v_mfma_f32_16x16x32_bf16 v[184:187], a[200:203], a[140:143], v[184:187]\n"
"ds_read_b64_tr_b16 a[210:211], v25 offset:12288\n"
"v_mfma_f32_16x16x32_bf16 v[200:203], a[200:203], a[144:147], v[200:203]\n"
"v_exp_f32 v91, -|v83|\n"
"v_mfma_f32_16x16x32_bf16 v[216:219], a[200:203], a[148:151], v[216:219]\n"
"ds_read_b64_tr_b16 a[212:213], v22 offset:12288\n"
"v_add_f32 v95, 1.0, v91\n"
"v_mfma_f32_16x16x32_bf16 v[232:235], a[200:203], a[152:155], v[232:235]\n"
"ds_read_b64_tr_b16 a[214:215], v26 offset:12288\n"
"v_mfma_f32_16x16x32_bf16 v[248:251], a[200:203], a[156:159], v[248:251]\n"
"v_exp_f32 v87, -v87\n"
"v_mfma_f32_16x16x32_bf16 v[140:143], a[204:207], a[128:131], v[140:143]\n"
"ds_read_b64_tr_b16 a[216:217], v23 offset:12288\n"
"v_fmac_f32 v95, v95, v87\n"
"v_mfma_f32_16x16x32_bf16 v[156:159], a[204:207], a[132:135], v[156:159]\n"
"ds_read_b64_tr_b16 a[218:219], v27 offset:12288\n"
"v_mfma_f32_16x16x32_bf16 v[172:175], a[204:207], a[136:139], v[172:175]\n"
"v_rcp_f32 v95, v95\n"
"v_mfma_f32_16x16x32_bf16 v[188:191], a[204:207], a[140:143], v[188:191]\n"
"ds_read_b64_tr_b16 a[220:221], v24 offset:12288\n"
"v_fma_f32 v91, -v91, v95, v95\n"
"v_mfma_f32_16x16x32_bf16 v[204:207], a[204:207], a[144:147], v[204:207]\n"
"ds_read_b64_tr_b16 a[222:223], v28 offset:12288\n"
"v_bfi_b32 v91, s70, v91, v83\n"
"v_mfma_f32_16x16x32_bf16 v[220:223], a[204:207], a[148:151], v[220:223]\n"
"v_exp_f32 v56, -|v48|\n"
"v_mfma_f32_16x16x32_bf16 v[236:239], a[204:207], a[152:155], v[236:239]\n"
"s_add_u32 m0, s62, 12288\n"
"s_add_u32 s61, s52, s53\n"
"v_mfma_f32_16x16x32_bf16 v[252:255], a[204:207], a[156:159], v[252:255]\n"
"s_waitcnt lgkmcnt(4)\n"
"v_cvt_pk_bf16_f32 v119, v90, v91\n"
"v_mfma_f32_16x16x32_bf16 v[128:131], a[208:211], a[160:163], v[128:131]\n"
"ds_read_b128 a[128:131], v18 offset:0\n"
"v_add_f32 v60, 1.0, v56\n"
"v_mfma_f32_16x16x32_bf16 v[144:147], a[208:211], a[164:167], v[144:147]\n"
"ds_read_b128 a[132:135], v18 offset:2048\n"
"v_accvgpr_read_b32 v80, a112\n"
"v_mfma_f32_16x16x32_bf16 v[160:163], a[208:211], a[168:171], v[160:163]\n"
"v_exp_f32 v52, -v52\n"
"v_mfma_f32_16x16x32_bf16 v[176:179], a[208:211], a[172:175], v[176:179]\n"
"ds_read_b128 a[136:139], v18 offset:4096\n"
"v_fmac_f32 v60, v60, v52\n"
"v_mfma_f32_16x16x32_bf16 v[192:195], a[208:211], a[176:179], v[192:195]\n"
"ds_read_b128 a[140:143], v18 offset:6144\n"
"v_accvgpr_read_b32 v84, a120\n"
"v_mfma_f32_16x16x32_bf16 v[208:211], a[208:211], a[180:183], v[208:211]\n"
"s_waitcnt lgkmcnt(4)\n"
"v_mul_f32 v80, s68, v80\n"
"v_mfma_f32_16x16x32_bf16 v[224:227], a[208:211], a[184:187], v[224:227]\n"
"buffer_load_dwordx4 v29, s[44:47], s61 offen offset:0 lds\n"
"v_mul_f32 v84, s69, v84\n"
"v_mfma_f32_16x16x32_bf16 v[240:243], a[208:211], a[188:191], v[240:243]\n"
"ds_read_b128 a[144:147], v18 offset:8192\n"
"v_accvgpr_read_b32 v81, a113\n"
"v_mfma_f32_16x16x32_bf16 v[132:135], a[212:215], a[160:163], v[132:135]\n"
"v_rcp_f32 v60, v60\n"
"v_mfma_f32_16x16x32_bf16 v[148:151], a[212:215], a[164:167], v[148:151]\n"
"buffer_load_dwordx4 v30, s[44:47], s61 offen offset:1024 lds\n"
"v_fma_f32 v56, -v56, v60, v60\n"
"v_mfma_f32_16x16x32_bf16 v[164:167], a[212:215], a[168:171], v[164:167]\n"
"ds_read_b128 a[148:151], v18 offset:10240\n"
"v_bfi_b32 v56, s70, v56, v48\n"
"v_mfma_f32_16x16x32_bf16 v[180:183], a[212:215], a[172:175], v[180:183]\n"
"v_exp_f32 v57, -|v49|\n"
"v_mfma_f32_16x16x32_bf16 v[196:199], a[212:215], a[176:179], v[196:199]\n"
"buffer_load_dwordx4 v31, s[44:47], s61 offen offset:2048 lds\n"
"v_add_f32 v61, 1.0, v57\n"
"v_mfma_f32_16x16x32_bf16 v[212:215], a[212:215], a[180:183], v[212:215]\n"
"ds_read_b128 a[152:155], v18 offset:12288\n"
"v_accvgpr_read_b32 v85, a121\n"
"v_mfma_f32_16x16x32_bf16 v[228:231], a[212:215], a[184:187], v[228:231]\n"
"v_exp_f32 v53, -v53\n"
"v_mfma_f32_16x16x32_bf16 v[244:247], a[212:215], a[188:191], v[244:247]\n"
"buffer_load_dwordx4 v32, s[44:47], s61 offen offset:3072 lds\n"
"v_fmac_f32 v61, v61, v53\n"
"v_mfma_f32_16x16x32_bf16 v[136:139], a[216:219], a[160:163], v[136:139]\n"
"ds_read_b128 a[156:159], v18 offset:14336\n"
"v_mul_f32 v81, s68, v81\n"
"v_mfma_f32_16x16x32_bf16 v[152:155], a[216:219], a[164:167], v[152:155]\n"
"s_waitcnt vmcnt(20)\n"
"v_mul_f32 v85, s69, v85\n"
"v_mfma_f32_16x16x32_bf16 v[168:171], a[216:219], a[168:171], v[168:171]\n"
"ds_read_b64_tr_b16 a[192:193], v21 offset:16384\n"
"v_accvgpr_read_b32 v82, a114\n"
"v_mfma_f32_16x16x32_bf16 v[184:187], a[216:219], a[172:175], v[184:187]\n"
"ds_read_b64_tr_b16 a[194:195], v25 offset:16384\n"
"v_accvgpr_read_b32 v86, a122\n"
"v_mfma_f32_16x16x32_bf16 v[200:203], a[216:219], a[176:179], v[200:203]\n"
"s_add_u32 s53, s53, 0x10000\n"
"s_and_b32 s53, s53, 0x3ffff\n"
"v_mfma_f32_16x16x32_bf16 v[216:219], a[216:219], a[180:183], v[216:219]\n"
"ds_read_b64_tr_b16 a[196:197], v22 offset:16384\n"
"v_mul_f32 v82, s68, v82\n"
"v_mfma_f32_16x16x32_bf16 v[232:235], a[216:219], a[184:187], v[232:235]\n"
"ds_read_b64_tr_b16 a[198:199], v26 offset:16384\n"
"v_mul_f32 v86, s69, v86\n"
"v_mfma_f32_16x16x32_bf16 v[248:251], a[216:219], a[188:191], v[248:251]\n"
"v_rcp_f32 v61, v61\n"
"v_mfma_f32_16x16x32_bf16 v[140:143], a[220:223], a[160:163], v[140:143]\n"
"ds_read_b64_tr_b16 a[200:201], v23 offset:16384\n"
"v_fma_f32 v57, -v57, v61, v61\n"
"v_mfma_f32_16x16x32_bf16 v[156:159], a[220:223], a[164:167], v[156:159]\n"
"ds_read_b64_tr_b16 a[202:203], v27 offset:16384\n"
"v_bfi_b32 v57, s70, v57, v49\n"
"v_mfma_f32_16x16x32_bf16 v[172:175], a[220:223], a[168:171], v[172:175]\n"
"v_exp_f32 v58, -|v50|\n"
"v_mfma_f32_16x16x32_bf16 v[188:191], a[220:223], a[172:175], v[188:191]\n"
"ds_read_b64_tr_b16 a[204:205], v24 offset:16384\n"
"v_add_f32 v62, 1.0, v58\n"
"v_mfma_f32_16x16x32_bf16 v[204:207], a[220:223], a[176:179], v[204:207]\n"
"ds_read_b64_tr_b16 a[206:207], v28 offset:16384\n"
"v_cvt_pk_bf16_f32 v120, v56, v57\n"
"v_mfma_f32_16x16x32_bf16 v[220:223], a[220:223], a[180:183], v[220:223]\n"
"v_exp_f32 v54, -v54\n"
"v_mfma_f32_16x16x32_bf16 v[236:239], a[220:223], a[184:187], v[236:239]\n"
"s_add_u32 m0, s62, 16384\n"
"s_add_u32 s61, s61, 0x8000\n"
"v_mfma_f32_16x16x32_bf16 v[252:255], a[220:223], a[188:191], v[252:255]\n"
"s_waitcnt lgkmcnt(4)\n"
"v_fmac_f32 v62, v62, v54\n"
"v_mfma_f32_16x16x32_bf16 v[128:131], a[192:195], a[128:131], v[128:131]\n"
"ds_read_b128 a[160:163], v18 offset:512\n"
"v_accvgpr_read_b32 v83, a115\n"
"v_mfma_f32_16x16x32_bf16 v[144:147], a[192:195], a[132:135], v[144:147]\n"
"ds_read_b128 a[164:167], v18 offset:2560\n"
"v_accvgpr_read_b32 v87, a123\n"
"v_mfma_f32_16x16x32_bf16 v[160:163], a[192:195], a[136:139], v[160:163]\n"
"v_rcp_f32 v62, v62\n"
"v_mfma_f32_16x16x32_bf16 v[176:179], a[192:195], a[140:143], v[176:179]\n"
"ds_read_b128 a[168:171], v18 offset:4608\n"
"v_fma_f32 v58, -v58, v62, v62\n"
"v_mfma_f32_16x16x32_bf16 v[192:195], a[192:195], a[144:147], v[192:195]\n"
"ds_read_b128 a[172:175], v18 offset:6656\n"
"v_bfi_b32 v58, s70, v58, v50\n"
"v_mfma_f32_16x16x32_bf16 v[208:211], a[192:195], a[148:151], v[208:211]\n"
"s_waitcnt lgkmcnt(4)\n"
"v_mul_f32 v83, s68, v83\n"
"v_mfma_f32_16x16x32_bf16 v[224:227], a[192:195], a[152:155], v[224:227]\n"
"buffer_load_dwordx4 v29, s[44:47], s61 offen offset:0 lds\n"
"v_mul_f32 v87, s69, v87\n"
"v_mfma_f32_16x16x32_bf16 v[240:243], a[192:195], a[156:159], v[240:243]\n"
"ds_read_b128 a[176:179], v18 offset:8704\n"
"v_mfma_f32_16x16x32_bf16 v[132:135], a[196:199], a[128:131], v[132:135]\n"
"v_exp_f32 v59, -|v51|\n"
"v_mfma_f32_16x16x32_bf16 v[148:151], a[196:199], a[132:135], v[148:151]\n"
"buffer_load_dwordx4 v30, s[44:47], s61 offen offset:1024 lds\n"
"v_add_f32 v63, 1.0, v59\n"
"v_mfma_f32_16x16x32_bf16 v[164:167], a[196:199], a[136:139], v[164:167]\n"
"ds_read_b128 a[180:183], v18 offset:10752\n"
"v_mfma_f32_16x16x32_bf16 v[180:183], a[196:199], a[140:143], v[180:183]\n"
"v_exp_f32 v55, -v55\n"
"v_mfma_f32_16x16x32_bf16 v[196:199], a[196:199], a[144:147], v[196:199]\n"
"buffer_load_dwordx4 v31, s[44:47], s61 offen offset:2048 lds\n"
"v_fmac_f32 v63, v63, v55\n"
"v_mfma_f32_16x16x32_bf16 v[212:215], a[196:199], a[148:151], v[212:215]\n"
"ds_read_b128 a[184:187], v18 offset:12800\n"
"v_mfma_f32_16x16x32_bf16 v[228:231], a[196:199], a[152:155], v[228:231]\n"
"v_rcp_f32 v63, v63\n"
"v_mfma_f32_16x16x32_bf16 v[244:247], a[196:199], a[156:159], v[244:247]\n"
"buffer_load_dwordx4 v32, s[44:47], s61 offen offset:3072 lds\n"
"v_fma_f32 v59, -v59, v63, v63\n"
"v_mfma_f32_16x16x32_bf16 v[136:139], a[200:203], a[128:131], v[136:139]\n"
"ds_read_b128 a[188:191], v18 offset:14848\n"
"v_bfi_b32 v59, s70, v59, v51\n"
"v_mfma_f32_16x16x32_bf16 v[152:155], a[200:203], a[132:135], v[152:155]\n"
"s_waitcnt vmcnt(12)\n"
"v_cvt_pk_bf16_f32 v121, v58, v59\n"
"v_mfma_f32_16x16x32_bf16 v[168:171], a[200:203], a[136:139], v[168:171]\n"
"ds_read_b64_tr_b16 a[208:209], v21 offset:0\n"
"v_add_u32 v18, 17408, v18\n"
"v_mfma_f32_16x16x32_bf16 v[184:187], a[200:203], a[140:143], v[184:187]\n"
"ds_read_b64_tr_b16 a[210:211], v25 offset:0\n"
"v_accvgpr_read_b32 v48, a116\n"
"v_mfma_f32_16x16x32_bf16 v[200:203], a[200:203], a[144:147], v[200:203]\n"
"v_exp_f32 v72, -|v64|\n"
"v_mfma_f32_16x16x32_bf16 v[216:219], a[200:203], a[148:151], v[216:219]\n"
"ds_read_b64_tr_b16 a[212:213], v22 offset:0\n"
"v_add_f32 v76, 1.0, v72\n"
"v_mfma_f32_16x16x32_bf16 v[232:235], a[200:203], a[152:155], v[232:235]\n"
"ds_read_b64_tr_b16 a[214:215], v26 offset:0\n"
"v_accvgpr_read_b32 v52, a124\n"
"v_mfma_f32_16x16x32_bf16 v[248:251], a[200:203], a[156:159], v[248:251]\n"
"v_exp_f32 v68, -v68\n"
"v_mfma_f32_16x16x32_bf16 v[140:143], a[204:207], a[128:131], v[140:143]\n"
"ds_read_b64_tr_b16 a[216:217], v23 offset:0\n"
"v_fmac_f32 v76, v76, v68\n"
"v_mfma_f32_16x16x32_bf16 v[156:159], a[204:207], a[132:135], v[156:159]\n"
"ds_read_b64_tr_b16 a[218:219], v27 offset:0\n"
"v_mul_f32 v48, s68, v48\n"
"v_mfma_f32_16x16x32_bf16 v[172:175], a[204:207], a[136:139], v[172:175]\n"
"v_rcp_f32 v76, v76\n"
"v_mfma_f32_16x16x32_bf16 v[188:191], a[204:207], a[140:143], v[188:191]\n"
"ds_read_b64_tr_b16 a[220:221], v24 offset:0\n"
"v_fma_f32 v72, -v72, v76, v76\n"
"v_mfma_f32_16x16x32_bf16 v[204:207], a[204:207], a[144:147], v[204:207]\n"
"ds_read_b64_tr_b16 a[222:223], v28 offset:0\n"
"v_bfi_b32 v72, s70, v72, v64\n"
"v_mfma_f32_16x16x32_bf16 v[220:223], a[204:207], a[148:151], v[220:223]\n"
"v_exp_f32 v73, -|v65|\n"
"v_mfma_f32_16x16x32_bf16 v[236:239], a[204:207], a[152:155], v[236:239]\n"
"s_add_u32 m0, s62, 0\n"
"s_add_u32 s61, s48, s53\n"
"v_mfma_f32_16x16x32_bf16 v[252:255], a[204:207], a[156:159], v[252:255]\n"
"s_waitcnt lgkmcnt(4)\n"
"v_add_f32 v77, 1.0, v73\n"
"v_mfma_f32_16x16x32_bf16 v[128:131], a[208:211], a[160:163], v[128:131]\n"
"ds_read_b128 a[128:131], v19 offset:0\n"
"v_mul_f32 v52, s69, v52\n"
"v_mfma_f32_16x16x32_bf16 v[144:147], a[208:211], a[164:167], v[144:147]\n"
"ds_read_b128 a[132:135], v19 offset:2048\n"
"v_accvgpr_read_b32 v49, a117\n"
"v_mfma_f32_16x16x32_bf16 v[160:163], a[208:211], a[168:171], v[160:163]\n"
"ds_read_b64_tr_b16 a[48:49], v37 offset:0\n"
"ds_read_b64_tr_b16 a[50:51], v39 offset:0\n"
"v_mfma_f32_16x16x32_bf16 v[176:179], a[208:211], a[172:175], v[176:179]\n"
"ds_read_b128 a[136:139], v19 offset:4096\n"
"v_accvgpr_read_b32 v53, a125\n"
"v_mfma_f32_16x16x32_bf16 v[192:195], a[208:211], a[176:179], v[192:195]\n"
"ds_read_b128 a[140:143], v19 offset:6144\n"
"v_mul_f32 v49, s68, v49\n"
"v_mfma_f32_16x16x32_bf16 v[208:211], a[208:211], a[180:183], v[208:211]\n"
"s_waitcnt lgkmcnt(6)\n"
"v_mul_f32 v53, s69, v53\n"
"v_mfma_f32_16x16x32_bf16 v[224:227], a[208:211], a[184:187], v[224:227]\n"
"buffer_load_dwordx4 v29, s[44:47], s61 offen offset:0 lds\n"
"v_accvgpr_read_b32 v50, a118\n"
"v_mfma_f32_16x16x32_bf16 v[240:243], a[208:211], a[188:191], v[240:243]\n"
"ds_read_b128 a[144:147], v19 offset:8192\n"
"v_accvgpr_read_b32 v54, a126\n"
"v_mfma_f32_16x16x32_bf16 v[132:135], a[212:215], a[160:163], v[132:135]\n"
"ds_read_b64_tr_b16 a[52:53], v38 offset:0\n"
"ds_read_b64_tr_b16 a[54:55], v40 offset:0\n"
"v_mfma_f32_16x16x32_bf16 v[148:151], a[212:215], a[164:167], v[148:151]\n"
"buffer_load_dwordx4 v30, s[44:47], s61 offen offset:1024 lds\n"
"v_mul_f32 v50, s68, v50\n"
"v_mfma_f32_16x16x32_bf16 v[164:167], a[212:215], a[168:171], v[164:167]\n"
"ds_read_b128 a[148:151], v19 offset:10240\n"
"v_mul_f32 v54, s69, v54\n"
"v_mfma_f32_16x16x32_bf16 v[180:183], a[212:215], a[172:175], v[180:183]\n"
"ds_read_b64_tr_b16 a[56:57], v37 offset:512\n"
"ds_read_b64_tr_b16 a[58:59], v39 offset:512\n"
"v_mfma_f32_16x16x32_bf16 v[196:199], a[212:215], a[176:179], v[196:199]\n"
"buffer_load_dwordx4 v31, s[44:47], s61 offen offset:2048 lds\n"
"v_accvgpr_read_b32 v51, a119\n"
"v_mfma_f32_16x16x32_bf16 v[212:215], a[212:215], a[180:183], v[212:215]\n"
"ds_read_b128 a[152:155], v19 offset:12288\n"
"v_accvgpr_read_b32 v55, a127\n"
"v_mfma_f32_16x16x32_bf16 v[228:231], a[212:215], a[184:187], v[228:231]\n"
"ds_read_b64_tr_b16 a[60:61], v38 offset:512\n"
"ds_read_b64_tr_b16 a[62:63], v40 offset:512\n"
"v_mfma_f32_16x16x32_bf16 v[244:247], a[212:215], a[188:191], v[244:247]\n"
"buffer_load_dwordx4 v32, s[44:47], s61 offen offset:3072 lds\n"
"s_xor_b32 s99, s98, 65536\n"
"v_mfma_f32_16x16x32_bf16 v[136:139], a[216:219], a[160:163], v[136:139]\n"
"ds_read_b128 a[156:159], v19 offset:14336\n"
"s_add_u32 s99, s99, 0\n"
"v_mfma_f32_16x16x32_bf16 v[152:155], a[216:219], a[164:167], v[152:155]\n"
"s_waitcnt vmcnt(20)\n"
"v_mul_f32 v51, s68, v51\n"
"v_mfma_f32_16x16x32_bf16 v[168:171], a[216:219], a[168:171], v[168:171]\n"
"ds_read_b64_tr_b16 a[192:193], v21 offset:4096\n"
"s_lshl_b32 m0, %[sm0], 2\n"
"v_mfma_f32_16x16x32_bf16 v[184:187], a[216:219], a[172:175], v[184:187]\n"
"ds_read_b64_tr_b16 a[194:195], v25 offset:4096\n"
"s_waitcnt lgkmcnt(3)\n"
"v_mfma_f32_16x16x32_bf16 v[200:203], a[216:219], a[176:179], v[200:203]\n"
"buffer_load_dwordx4 v33, s[64:67], s99 offen offset:0 lds\n"
"v_mul_f32 v55, s69, v55\n"
"v_mfma_f32_16x16x32_bf16 v[216:219], a[216:219], a[180:183], v[216:219]\n"
"ds_read_b64_tr_b16 a[196:197], v22 offset:4096\n"
"v_mfma_f32_16x16x32_bf16 v[232:235], a[216:219], a[184:187], v[232:235]\n"
"ds_read_b64_tr_b16 a[198:199], v26 offset:4096\n"
"v_mfma_f32_16x16x32_bf16 v[248:251], a[216:219], a[188:191], v[248:251]\n"
"buffer_load_dwordx4 v34, s[64:67], s99 offen offset:1024 lds\n"
"v_mfma_f32_16x16x32_bf16 v[140:143], a[220:223], a[160:163], v[140:143]\n"
"ds_read_b64_tr_b16 a[200:201], v23 offset:4096\n"
"v_mfma_f32_16x16x32_bf16 v[156:159], a[220:223], a[164:167], v[156:159]\n"
"ds_read_b64_tr_b16 a[202:203], v27 offset:4096\n"
"v_mfma_f32_16x16x32_bf16 v[172:175], a[220:223], a[168:171], v[172:175]\n"
"buffer_load_dwordx4 v35, s[64:67], s99 offen offset:2048 lds\n"
"v_mfma_f32_16x16x32_bf16 v[188:191], a[220:223], a[172:175], v[188:191]\n"
"ds_read_b64_tr_b16 a[204:205], v24 offset:4096\n"
"v_mfma_f32_16x16x32_bf16 v[204:207], a[220:223], a[176:179], v[204:207]\n"
"ds_read_b64_tr_b16 a[206:207], v28 offset:4096\n"
"v_mfma_f32_16x16x32_bf16 v[220:223], a[220:223], a[180:183], v[220:223]\n"
"buffer_load_dwordx4 v36, s[64:67], s99 offen offset:3072 lds\n"
"v_mfma_f32_16x16x32_bf16 v[236:239], a[220:223], a[184:187], v[236:239]\n"
"s_add_u32 m0, s62, 4096\n"
"s_add_u32 s61, s61, 0x8000\n"
"v_mfma_f32_16x16x32_bf16 v[252:255], a[220:223], a[188:191], v[252:255]\n"
"s_waitcnt lgkmcnt(4)\n"
"v_mfma_f32_16x16x32_bf16 v[128:131], a[192:195], a[128:131], v[128:131]\n"
"ds_read_b128 a[160:163], v19 offset:512\n"
"v_mfma_f32_16x16x32_bf16 v[144:147], a[192:195], a[132:135], v[144:147]\n"
"ds_read_b128 a[164:167], v19 offset:2560\n"
"v_mfma_f32_16x16x32_bf16 v[160:163], a[192:195], a[136:139], v[160:163]\n"
"v_exp_f32 v69, -v69\n"
"v_mfma_f32_16x16x32_bf16 v[176:179], a[192:195], a[140:143], v[176:179]\n"
"ds_read_b128 a[168:171], v19 offset:4608\n"
"v_fmac_f32 v77, v77, v69\n"
"v_mfma_f32_16x16x32_bf16 v[192:195], a[192:195], a[144:147], v[192:195]\n"
"ds_read_b128 a[172:175], v19 offset:6656\n"
"v_mfma_f32_16x16x32_bf16 v[208:211], a[192:195], a[148:151], v[208:211]\n"
"s_waitcnt lgkmcnt(4)\n"
"v_mfma_f32_16x16x32_bf16 v[224:227], a[192:195], a[152:155], v[224:227]\n"
"buffer_load_dwordx4 v29, s[44:47], s61 offen offset:0 lds\n"
"v_mfma_f32_16x16x32_bf16 v[240:243], a[192:195], a[156:159], v[240:243]\n"
"ds_read_b128 a[176:179], v19 offset:8704\n"
"v_mfma_f32_16x16x32_bf16 v[132:135], a[196:199], a[128:131], v[132:135]\n"
"v_rcp_f32 v77, v77\n"
"v_mfma_f32_16x16x32_bf16 v[148:151], a[196:199], a[132:135], v[148:151]\n"
"buffer_load_dwordx4 v30, s[44:47], s61 offen offset:1024 lds\n"
"v_fma_f32 v73, -v73, v77, v77\n"
"v_mfma_f32_16x16x32_bf16 v[164:167], a[196:199], a[136:139], v[164:167]\n"
"ds_read_b128 a[180:183], v19 offset:10752\n"
"v_bfi_b32 v73, s70, v73, v65\n"
"v_mfma_f32_16x16x32_bf16 v[180:183], a[196:199], a[140:143], v[180:183]\n"
"v_exp_f32 v74, -|v66|\n"
"v_mfma_f32_16x16x32_bf16 v[196:199], a[196:199], a[144:147], v[196:199]\n"
"buffer_load_dwordx4 v31, s[44:47], s61 offen offset:2048 lds\n"
"v_add_f32 v78, 1.0, v74\n"
"v_mfma_f32_16x16x32_bf16 v[212:215], a[196:199], a[148:151], v[212:215]\n"
"ds_read_b128 a[184:187], v19 offset:12800\n"
"v_cvt_pk_bf16_f32 v122, v72, v73\n"
"v_mfma_f32_16x16x32_bf16 v[228:231], a[196:199], a[152:155], v[228:231]\n"
"v_exp_f32 v70, -v70\n"
"v_mfma_f32_16x16x32_bf16 v[244:247], a[196:199], a[156:159], v[244:247]\n"
"buffer_load_dwordx4 v32, s[44:47], s61 offen offset:3072 lds\n"
"v_fmac_f32 v78, v78, v70\n"
"v_mfma_f32_16x16x32_bf16 v[136:139], a[200:203], a[128:131], v[136:139]\n"
"ds_read_b128 a[188:191], v19 offset:14848\n"
"v_mfma_f32_16x16x32_bf16 v[152:155], a[200:203], a[132:135], v[152:155]\n"
"s_waitcnt vmcnt(20)\n"
"v_mfma_f32_16x16x32_bf16 v[168:171], a[200:203], a[136:139], v[168:171]\n"
"ds_read_b64_tr_b16 a[208:209], v21 offset:8192\n"
"v_add_u32 v19, 17408, v19\n"
"v_mfma_f32_16x16x32_bf16 v[184:187], a[200:203], a[140:143], v[184:187]\n"
"ds_read_b64_tr_b16 a[210:211], v25 offset:8192\n"
"v_mfma_f32_16x16x32_bf16 v[200:203], a[200:203], a[144:147], v[200:203]\n"
"v_rcp_f32 v78, v78\n"
"v_mfma_f32_16x16x32_bf16 v[216:219], a[200:203], a[148:151], v[216:219]\n"
"ds_read_b64_tr_b16 a[212:213], v22 offset:8192\n"
"v_fma_f32 v74, -v74, v78, v78\n"
"v_mfma_f32_16x16x32_bf16 v[232:235], a[200:203], a[152:155], v[232:235]\n"
"ds_read_b64_tr_b16 a[214:215], v26 offset:8192\n"
"v_bfi_b32 v74, s70, v74, v66\n"
"v_mfma_f32_16x16x32_bf16 v[248:251], a[200:203], a[156:159], v[248:251]\n"
"v_exp_f32 v75, -|v67|\n"
"v_mfma_f32_16x16x32_bf16 v[140:143], a[204:207], a[128:131], v[140:143]\n"
"ds_read_b64_tr_b16 a[216:217], v23 offset:8192\n"
"v_add_f32 v79, 1.0, v75\n"
"v_mfma_f32_16x16x32_bf16 v[156:159], a[204:207], a[132:135], v[156:159]\n"
"ds_read_b64_tr_b16 a[218:219], v27 offset:8192\n"
"v_mfma_f32_16x16x32_bf16 v[172:175], a[204:207], a[136:139], v[172:175]\n"
"v_exp_f32 v71, -v71\n"
"v_mfma_f32_16x16x32_bf16 v[188:191], a[204:207], a[140:143], v[188:191]\n"
"ds_read_b64_tr_b16 a[220:221], v24 offset:8192\n"
"v_fmac_f32 v79, v79, v71\n"
"v_mfma_f32_16x16x32_bf16 v[204:207], a[204:207], a[144:147], v[204:207]\n"
"ds_read_b64_tr_b16 a[222:223], v28 offset:8192\n"
"v_mfma_f32_16x16x32_bf16 v[220:223], a[204:207], a[148:151], v[220:223]\n"
"v_rcp_f32 v79, v79\n"
"v_mfma_f32_16x16x32_bf16 v[236:239], a[204:207], a[152:155], v[236:239]\n"
"s_add_u32 m0, s62, 8192\n"
"s_add_u32 s61, s49, s53\n"
"v_mfma_f32_16x16x32_bf16 v[252:255], a[204:207], a[156:159], v[252:255]\n"
"s_waitcnt lgkmcnt(4)\n"
"v_fma_f32 v75, -v75, v79, v79\n"
"v_mfma_f32_16x16x32_bf16 v[128:131], a[208:211], a[160:163], v[128:131]\n"
"ds_read_b128 a[128:131], v20 offset:0\n"
"v_bfi_b32 v75, s70, v75, v67\n"
"v_mfma_f32_16x16x32_bf16 v[144:147], a[208:211], a[164:167], v[144:147]\n"
"ds_read_b128 a[132:135], v20 offset:2048\n"
"v_cvt_pk_bf16_f32 v123, v74, v75\n"
"v_mfma_f32_16x16x32_bf16 v[160:163], a[208:211], a[168:171], v[160:163]\n"
"v_exp_f32 v88, -|v80|\n"
"v_mfma_f32_16x16x32_bf16 v[176:179], a[208:211], a[172:175], v[176:179]\n"
"ds_read_b128 a[136:139], v20 offset:4096\n"
"v_add_f32 v92, 1.0, v88\n"
"v_mfma_f32_16x16x32_bf16 v[192:195], a[208:211], a[176:179], v[192:195]\n"
"ds_read_b128 a[140:143], v20 offset:6144\n"
"v_mfma_f32_16x16x32_bf16 v[208:211], a[208:211], a[180:183], v[208:211]\n"
"s_waitcnt lgkmcnt(4)\n"
"v_mfma_f32_16x16x32_bf16 v[224:227], a[208:211], a[184:187], v[224:227]\n"
"buffer_load_dwordx4 v29, s[44:47], s61 offen offset:0 lds\n"
"v_mfma_f32_16x16x32_bf16 v[240:243], a[208:211], a[188:191], v[240:243]\n"
"ds_read_b128 a[144:147], v20 offset:8192\n"
"v_mfma_f32_16x16x32_bf16 v[132:135], a[212:215], a[160:163], v[132:135]\n"
"v_exp_f32 v84, -v84\n"
"v_mfma_f32_16x16x32_bf16 v[148:151], a[212:215], a[164:167], v[148:151]\n"
"buffer_load_dwordx4 v30, s[44:47], s61 offen offset:1024 lds\n"
"v_fmac_f32 v92, v92, v84\n"
"v_mfma_f32_16x16x32_bf16 v[164:167], a[212:215], a[168:171], v[164:167]\n"
"ds_read_b128 a[148:151], v20 offset:10240\n"
"v_mfma_f32_16x16x32_bf16 v[180:183], a[212:215], a[172:175], v[180:183]\n"
"v_rcp_f32 v92, v92\n"
"v_mfma_f32_16x16x32_bf16 v[196:199], a[212:215], a[176:179], v[196:199]\n"
"buffer_load_dwordx4 v31, s[44:47], s61 offen offset:2048 lds\n"
"v_fma_f32 v88, -v88, v92, v92\n"
"v_mfma_f32_16x16x32_bf16 v[212:215], a[212:215], a[180:183], v[212:215]\n"
"ds_read_b128 a[152:155], v20 offset:12288\n"
"v_bfi_b32 v88, s70, v88, v80\n"
"v_mfma_f32_16x16x32_bf16 v[228:231], a[212:215], a[184:187], v[228:231]\n"
"v_exp_f32 v89, -|v81|\n"
"v_mfma_f32_16x16x32_bf16 v[244:247], a[212:215], a[188:191], v[244:247]\n"
"buffer_load_dwordx4 v32, s[44:47], s61 offen offset:3072 lds\n"
"v_add_f32 v93, 1.0, v89\n"
"v_mfma_f32_16x16x32_bf16 v[136:139], a[216:219], a[160:163], v[136:139]\n"
"ds_read_b128 a[156:159], v20 offset:14336\n"
"v_mfma_f32_16x16x32_bf16 v[152:155], a[216:219], a[164:167], v[152:155]\n"
"s_waitcnt vmcnt(20)\n"
"v_mfma_f32_16x16x32_bf16 v[168:171], a[216:219], a[168:171], v[168:171]\n"
"ds_read_b64_tr_b16 a[192:193], v21 offset:12288\n"
"v_mfma_f32_16x16x32_bf16 v[184:187], a[216:219], a[172:175], v[184:187]\n"
"ds_read_b64_tr_b16 a[194:195], v25 offset:12288\n"
"v_mfma_f32_16x16x32_bf16 v[200:203], a[216:219], a[176:179], v[200:203]\n"
"v_exp_f32 v85, -v85\n"
"v_mfma_f32_16x16x32_bf16 v[216:219], a[216:219], a[180:183], v[216:219]\n"
"ds_read_b64_tr_b16 a[196:197], v22 offset:12288\n"
"v_fmac_f32 v93, v93, v85\n"
"v_mfma_f32_16x16x32_bf16 v[232:235], a[216:219], a[184:187], v[232:235]\n"
"ds_read_b64_tr_b16 a[198:199], v26 offset:12288\n"
"v_mfma_f32_16x16x32_bf16 v[248:251], a[216:219], a[188:191], v[248:251]\n"
"v_rcp_f32 v93, v93\n"
"v_mfma_f32_16x16x32_bf16 v[140:143], a[220:223], a[160:163], v[140:143]\n"
"ds_read_b64_tr_b16 a[200:201], v23 offset:12288\n"
"v_fma_f32 v89, -v89, v93, v93\n"
"v_mfma_f32_16x16x32_bf16 v[156:159], a[220:223], a[164:167], v[156:159]\n"
"ds_read_b64_tr_b16 a[202:203], v27 offset:12288\n"
"v_bfi_b32 v89, s70, v89, v81\n"
"v_mfma_f32_16x16x32_bf16 v[172:175], a[220:223], a[168:171], v[172:175]\n"
"v_exp_f32 v90, -|v82|\n"
"v_mfma_f32_16x16x32_bf16 v[188:191], a[220:223], a[172:175], v[188:191]\n"
"ds_read_b64_tr_b16 a[204:205], v24 offset:12288\n"
"v_add_f32 v94, 1.0, v90\n"
"v_mfma_f32_16x16x32_bf16 v[204:207], a[220:223], a[176:179], v[204:207]\n"
"ds_read_b64_tr_b16 a[206:207], v28 offset:12288\n"
"v_cvt_pk_bf16_f32 v124, v88, v89\n"
"v_mfma_f32_16x16x32_bf16 v[220:223], a[220:223], a[180:183], v[220:223]\n"
"v_exp_f32 v86, -v86\n"
"v_mfma_f32_16x16x32_bf16 v[236:239], a[220:223], a[184:187], v[236:239]\n"
"s_add_u32 m0, s62, 12288\n"
"s_add_u32 s61, s61, 0x8000\n"
"v_mfma_f32_16x16x32_bf16 v[252:255], a[220:223], a[188:191], v[252:255]\n"
"s_waitcnt lgkmcnt(4)\n"
"v_fmac_f32 v94, v94, v86\n"
"v_mfma_f32_16x16x32_bf16 v[128:131], a[192:195], a[128:131], v[128:131]\n"
"ds_read_b128 a[160:163], v20 offset:512\n"
"v_mfma_f32_16x16x32_bf16 v[144:147], a[192:195], a[132:135], v[144:147]\n"
"ds_read_b128 a[164:167], v20 offset:2560\n"
"v_mfma_f32_16x16x32_bf16 v[160:163], a[192:195], a[136:139], v[160:163]\n"
"v_rcp_f32 v94, v94\n"
"v_mfma_f32_16x16x32_bf16 v[176:179], a[192:195], a[140:143], v[176:179]\n"
"ds_read_b128 a[168:171], v20 offset:4608\n"
"v_fma_f32 v90, -v90, v94, v94\n"
"v_mfma_f32_16x16x32_bf16 v[192:195], a[192:195], a[144:147], v[192:195]\n"
"ds_read_b128 a[172:175], v20 offset:6656\n"
"v_bfi_b32 v90, s70, v90, v82\n"
"v_mfma_f32_16x16x32_bf16 v[208:211], a[192:195], a[148:151], v[208:211]\n"
"s_waitcnt lgkmcnt(4)\n"
"v_mfma_f32_16x16x32_bf16 v[224:227], a[192:195], a[152:155], v[224:227]\n"
"buffer_load_dwordx4 v29, s[44:47], s61 offen offset:0 lds\n"
"v_mfma_f32_16x16x32_bf16 v[240:243], a[192:195], a[156:159], v[240:243]\n"
"ds_read_b128 a[176:179], v20 offset:8704\n"
"v_mfma_f32_16x16x32_bf16 v[132:135], a[196:199], a[128:131], v[132:135]\n"
"v_exp_f32 v91, -|v83|\n"
"v_mfma_f32_16x16x32_bf16 v[148:151], a[196:199], a[132:135], v[148:151]\n"
"buffer_load_dwordx4 v30, s[44:47], s61 offen offset:1024 lds\n"
"v_add_f32 v95, 1.0, v91\n"
"v_mfma_f32_16x16x32_bf16 v[164:167], a[196:199], a[136:139], v[164:167]\n"
"ds_read_b128 a[180:183], v20 offset:10752\n"
"v_mfma_f32_16x16x32_bf16 v[180:183], a[196:199], a[140:143], v[180:183]\n"
"v_exp_f32 v87, -v87\n"
"v_mfma_f32_16x16x32_bf16 v[196:199], a[196:199], a[144:147], v[196:199]\n"
"buffer_load_dwordx4 v31, s[44:47], s61 offen offset:2048 lds\n"
"v_fmac_f32 v95, v95, v87\n"
"v_mfma_f32_16x16x32_bf16 v[212:215], a[196:199], a[148:151], v[212:215]\n"
"ds_read_b128 a[184:187], v20 offset:12800\n"
"v_mfma_f32_16x16x32_bf16 v[228:231], a[196:199], a[152:155], v[228:231]\n"
"v_rcp_f32 v95, v95\n"
"v_mfma_f32_16x16x32_bf16 v[244:247], a[196:199], a[156:159], v[244:247]\n"
"buffer_load_dwordx4 v32, s[44:47], s61 offen offset:3072 lds\n"
"v_fma_f32 v91, -v91, v95, v95\n"
"v_mfma_f32_16x16x32_bf16 v[136:139], a[200:203], a[128:131], v[136:139]\n"
"ds_read_b128 a[188:191], v20 offset:14848\n"
"v_bfi_b32 v91, s70, v91, v83\n"
"v_mfma_f32_16x16x32_bf16 v[152:155], a[200:203], a[132:135], v[152:155]\n"
"s_waitcnt vmcnt(12)\n"
"v_cvt_pk_bf16_f32 v125, v90, v91\n"
"v_mfma_f32_16x16x32_bf16 v[168:171], a[200:203], a[136:139], v[168:171]\n"
"ds_read_b64_tr_b16 a[208:209], v21 offset:16384\n"
"v_add_u32 v20, 17408, v20\n"
"v_mfma_f32_16x16x32_bf16 v[184:187], a[200:203], a[140:143], v[184:187]\n"
"ds_read_b64_tr_b16 a[210:211], v25 offset:16384\n"
"v_mfma_f32_16x16x32_bf16 v[200:203], a[200:203], a[144:147], v[200:203]\n"
"v_exp_f32 v56, -|v48|\n"
"v_mfma_f32_16x16x32_bf16 v[216:219], a[200:203], a[148:151], v[216:219]\n"
"ds_read_b64_tr_b16 a[212:213], v22 offset:16384\n"
"v_add_f32 v60, 1.0, v56\n"
"v_mfma_f32_16x16x32_bf16 v[232:235], a[200:203], a[152:155], v[232:235]\n"
"ds_read_b64_tr_b16 a[214:215], v26 offset:16384\n"
"v_mfma_f32_16x16x32_bf16 v[248:251], a[200:203], a[156:159], v[248:251]\n"
"v_exp_f32 v52, -v52\n"
"v_mfma_f32_16x16x32_bf16 v[140:143], a[204:207], a[128:131], v[140:143]\n"
"ds_read_b64_tr_b16 a[216:217], v23 offset:16384\n"
"v_fmac_f32 v60, v60, v52\n"
"v_mfma_f32_16x16x32_bf16 v[156:159], a[204:207], a[132:135], v[156:159]\n"
"ds_read_b64_tr_b16 a[218:219], v27 offset:16384\n"
"v_mfma_f32_16x16x32_bf16 v[172:175], a[204:207], a[136:139], v[172:175]\n"
"v_rcp_f32 v60, v60\n"
"v_mfma_f32_16x16x32_bf16 v[188:191], a[204:207], a[140:143], v[188:191]\n"
"ds_read_b64_tr_b16 a[220:221], v24 offset:16384\n"
"v_fma_f32 v56, -v56, v60, v60\n"
"v_mfma_f32_16x16x32_bf16 v[204:207], a[204:207], a[144:147], v[204:207]\n"
"ds_read_b64_tr_b16 a[222:223], v28 offset:16384\n"
"v_bfi_b32 v56, s70, v56, v48\n"
"v_mfma_f32_16x16x32_bf16 v[220:223], a[204:207], a[148:151], v[220:223]\n"
"v_exp_f32 v57, -|v49|\n"
"v_mfma_f32_16x16x32_bf16 v[236:239], a[204:207], a[152:155], v[236:239]\n"
"s_add_u32 m0, s62, 16384\n"
"s_add_u32 s61, s50, s53\n"
"v_mfma_f32_16x16x32_bf16 v[252:255], a[204:207], a[156:159], v[252:255]\n"
"s_waitcnt lgkmcnt(4)\n"
"v_add_f32 v61, 1.0, v57\n"
"v_mfma_f32_16x16x32_bf16 v[128:131], a[208:211], a[160:163], v[128:131]\n"
"ds_read_b128 a[128:131], v16 offset:0\n"
"v_mfma_f32_16x16x32_bf16 v[144:147], a[208:211], a[164:167], v[144:147]\n"
"ds_read_b128 a[132:135], v16 offset:2048\n"
"v_mfma_f32_16x16x32_bf16 v[160:163], a[208:211], a[168:171], v[160:163]\n"
"ds_read_b64_tr_b16 a[64:65], v37 offset:0\n"
"ds_read_b64_tr_b16 a[66:67], v39 offset:0\n"
"v_mfma_f32_16x16x32_bf16 v[176:179], a[208:211], a[172:175], v[176:179]\n"
"ds_read_b128 a[136:139], v16 offset:4096\n"
"v_mfma_f32_16x16x32_bf16 v[192:195], a[208:211], a[176:179], v[192:195]\n"
"ds_read_b128 a[140:143], v16 offset:6144\n"
"v_mfma_f32_16x16x32_bf16 v[208:211], a[208:211], a[180:183], v[208:211]\n"
"s_waitcnt lgkmcnt(6)\n"
"v_mfma_f32_16x16x32_bf16 v[224:227], a[208:211], a[184:187], v[224:227]\n"
"buffer_load_dwordx4 v29, s[44:47], s61 offen offset:0 lds\n"
"v_mfma_f32_16x16x32_bf16 v[240:243], a[208:211], a[188:191], v[240:243]\n"
"ds_read_b128 a[144:147], v16 offset:8192\n"
"v_mfma_f32_16x16x32_bf16 v[132:135], a[212:215], a[160:163], v[132:135]\n"
"ds_read_b64_tr_b16 a[68:69], v38 offset:0\n"
"ds_read_b64_tr_b16 a[70:71], v40 offset:0\n"
"v_mfma_f32_16x16x32_bf16 v[148:151], a[212:215], a[164:167], v[148:151]\n"
"buffer_load_dwordx4 v30, s[44:47], s61 offen offset:1024 lds\n"
"v_mfma_f32_16x16x32_bf16 v[164:167], a[212:215], a[168:171], v[164:167]\n"
"ds_read_b128 a[148:151], v16 offset:10240\n"
"v_mfma_f32_16x16x32_bf16 v[180:183], a[212:215], a[172:175], v[180:183]\n"
"ds_read_b64_tr_b16 a[72:73], v37 offset:512\n"
"ds_read_b64_tr_b16 a[74:75], v39 offset:512\n"
"v_mfma_f32_16x16x32_bf16 v[196:199], a[212:215], a[176:179], v[196:199]\n"
"buffer_load_dwordx4 v31, s[44:47], s61 offen offset:2048 lds\n"
"v_mfma_f32_16x16x32_bf16 v[212:215], a[212:215], a[180:183], v[212:215]\n"
"ds_read_b128 a[152:155], v16 offset:12288\n"
"v_mfma_f32_16x16x32_bf16 v[228:231], a[212:215], a[184:187], v[228:231]\n"
"ds_read_b64_tr_b16 a[76:77], v38 offset:512\n"
"ds_read_b64_tr_b16 a[78:79], v40 offset:512\n"
"v_mfma_f32_16x16x32_bf16 v[244:247], a[212:215], a[188:191], v[244:247]\n"
"buffer_load_dwordx4 v32, s[44:47], s61 offen offset:3072 lds\n"
"s_xor_b32 s99, s98, 65536\n"
"v_mfma_f32_16x16x32_bf16 v[136:139], a[216:219], a[160:163], v[136:139]\n"
"ds_read_b128 a[156:159], v16 offset:14336\n"
"s_add_u32 s99, s99, 16384\n"
"v_mfma_f32_16x16x32_bf16 v[152:155], a[216:219], a[164:167], v[152:155]\n"
"s_waitcnt vmcnt(20)\n"
"v_mfma_f32_16x16x32_bf16 v[168:171], a[216:219], a[168:171], v[168:171]\n"
"ds_read_b64_tr_b16 a[192:193], v21 offset:0\n"
"s_lshl_b32 m0, %[sm0], 2\n"
"v_mfma_f32_16x16x32_bf16 v[184:187], a[216:219], a[172:175], v[184:187]\n"
"ds_read_b64_tr_b16 a[194:195], v25 offset:0\n"
"s_waitcnt lgkmcnt(3)\n"
"v_mfma_f32_16x16x32_bf16 v[200:203], a[216:219], a[176:179], v[200:203]\n"
"buffer_load_dwordx4 v33, s[64:67], s99 offen offset:0 lds\n"
"v_mfma_f32_16x16x32_bf16 v[216:219], a[216:219], a[180:183], v[216:219]\n"
"ds_read_b64_tr_b16 a[196:197], v22 offset:0\n"
"v_mfma_f32_16x16x32_bf16 v[232:235], a[216:219], a[184:187], v[232:235]\n"
"ds_read_b64_tr_b16 a[198:199], v26 offset:0\n"
"v_mfma_f32_16x16x32_bf16 v[248:251], a[216:219], a[188:191], v[248:251]\n"
"buffer_load_dwordx4 v34, s[64:67], s99 offen offset:1024 lds\n"
"v_mfma_f32_16x16x32_bf16 v[140:143], a[220:223], a[160:163], v[140:143]\n"
"ds_read_b64_tr_b16 a[200:201], v23 offset:0\n"
"v_mfma_f32_16x16x32_bf16 v[156:159], a[220:223], a[164:167], v[156:159]\n"
"ds_read_b64_tr_b16 a[202:203], v27 offset:0\n"
"v_mfma_f32_16x16x32_bf16 v[172:175], a[220:223], a[168:171], v[172:175]\n"
"buffer_load_dwordx4 v35, s[64:67], s99 offen offset:2048 lds\n"
"v_mfma_f32_16x16x32_bf16 v[188:191], a[220:223], a[172:175], v[188:191]\n"
"ds_read_b64_tr_b16 a[204:205], v24 offset:0\n"
"v_mfma_f32_16x16x32_bf16 v[204:207], a[220:223], a[176:179], v[204:207]\n"
"ds_read_b64_tr_b16 a[206:207], v28 offset:0\n"
"v_mfma_f32_16x16x32_bf16 v[220:223], a[220:223], a[180:183], v[220:223]\n"
"buffer_load_dwordx4 v36, s[64:67], s99 offen offset:3072 lds\n"
"v_mfma_f32_16x16x32_bf16 v[236:239], a[220:223], a[184:187], v[236:239]\n"
"s_add_u32 m0, s62, 0\n"
"s_add_u32 s61, s61, 0x8000\n"
"v_mfma_f32_16x16x32_bf16 v[252:255], a[220:223], a[188:191], v[252:255]\n"
"s_waitcnt lgkmcnt(4)\n"
"v_mfma_f32_16x16x32_bf16 v[128:131], a[192:195], a[128:131], v[128:131]\n"
"ds_read_b128 a[160:163], v16 offset:512\n"
"v_mfma_f32_16x16x32_bf16 v[144:147], a[192:195], a[132:135], v[144:147]\n"
"ds_read_b128 a[164:167], v16 offset:2560\n"
"v_mfma_f32_16x16x32_bf16 v[160:163], a[192:195], a[136:139], v[160:163]\n"
"v_exp_f32 v53, -v53\n"
"v_mfma_f32_16x16x32_bf16 v[176:179], a[192:195], a[140:143], v[176:179]\n"
"ds_read_b128 a[168:171], v16 offset:4608\n"
"v_fmac_f32 v61, v61, v53\n"
"v_mfma_f32_16x16x32_bf16 v[192:195], a[192:195], a[144:147], v[192:195]\n"
"ds_read_b128 a[172:175], v16 offset:6656\n"
"v_mfma_f32_16x16x32_bf16 v[208:211], a[192:195], a[148:151], v[208:211]\n"
"s_waitcnt lgkmcnt(4)\n"
"v_mfma_f32_16x16x32_bf16 v[224:227], a[192:195], a[152:155], v[224:227]\n"
"buffer_load_dwordx4 v29, s[44:47], s61 offen offset:0 lds\n"
"v_mfma_f32_16x16x32_bf16 v[240:243], a[192:195], a[156:159], v[240:243]\n"
"ds_read_b128 a[176:179], v16 offset:8704\n"
"v_mfma_f32_16x16x32_bf16 v[132:135], a[196:199], a[128:131], v[132:135]\n"
"v_rcp_f32 v61, v61\n"
"v_mfma_f32_16x16x32_bf16 v[148:151], a[196:199], a[132:135], v[148:151]\n"
"buffer_load_dwordx4 v30, s[44:47], s61 offen offset:1024 lds\n"
"v_fma_f32 v57, -v57, v61, v61\n"
"v_mfma_f32_16x16x32_bf16 v[164:167], a[196:199], a[136:139], v[164:167]\n"
"ds_read_b128 a[180:183], v16 offset:10752\n"
"v_bfi_b32 v57, s70, v57, v49\n"
"v_mfma_f32_16x16x32_bf16 v[180:183], a[196:199], a[140:143], v[180:183]\n"
"v_exp_f32 v58, -|v50|\n"
"v_mfma_f32_16x16x32_bf16 v[196:199], a[196:199], a[144:147], v[196:199]\n"
"buffer_load_dwordx4 v31, s[44:47], s61 offen offset:2048 lds\n"
"v_add_f32 v62, 1.0, v58\n"
"v_mfma_f32_16x16x32_bf16 v[212:215], a[196:199], a[148:151], v[212:215]\n"
"ds_read_b128 a[184:187], v16 offset:12800\n"
"v_cvt_pk_bf16_f32 v126, v56, v57\n"
"v_mfma_f32_16x16x32_bf16 v[228:231], a[196:199], a[152:155], v[228:231]\n"
"v_exp_f32 v54, -v54\n"
"v_mfma_f32_16x16x32_bf16 v[244:247], a[196:199], a[156:159], v[244:247]\n"
"buffer_load_dwordx4 v32, s[44:47], s61 offen offset:3072 lds\n"
"v_fmac_f32 v62, v62, v54\n"
"v_mfma_f32_16x16x32_bf16 v[136:139], a[200:203], a[128:131], v[136:139]\n"
"ds_read_b128 a[188:191], v16 offset:14848\n"
"v_mfma_f32_16x16x32_bf16 v[152:155], a[200:203], a[132:135], v[152:155]\n"
"s_waitcnt vmcnt(20)\n"
"v_mfma_f32_16x16x32_bf16 v[168:171], a[200:203], a[136:139], v[168:171]\n"
"ds_read_b64_tr_b16 a[208:209], v21 offset:4096\n"
"v_add_u32 v16, 17408, v16\n"
"v_mfma_f32_16x16x32_bf16 v[184:187], a[200:203], a[140:143], v[184:187]\n"
"ds_read_b64_tr_b16 a[210:211], v25 offset:4096\n"
"v_mfma_f32_16x16x32_bf16 v[200:203], a[200:203], a[144:147], v[200:203]\n"
"v_rcp_f32 v62, v62\n"
"v_mfma_f32_16x16x32_bf16 v[216:219], a[200:203], a[148:151], v[216:219]\n"
"ds_read_b64_tr_b16 a[212:213], v22 offset:4096\n"
"v_fma_f32 v58, -v58, v62, v62\n"
"v_mfma_f32_16x16x32_bf16 v[232:235], a[200:203], a[152:155], v[232:235]\n"
"ds_read_b64_tr_b16 a[214:215], v26 offset:4096\n"
"v_bfi_b32 v58, s70, v58, v50\n"
"v_mfma_f32_16x16x32_bf16 v[248:251], a[200:203], a[156:159], v[248:251]\n"
"v_exp_f32 v59, -|v51|\n"
"v_mfma_f32_16x16x32_bf16 v[140:143], a[204:207], a[128:131], v[140:143]\n"
"ds_read_b64_tr_b16 a[216:217], v23 offset:4096\n"
"v_add_f32 v63, 1.0, v59\n"
"v_mfma_f32_16x16x32_bf16 v[156:159], a[204:207], a[132:135], v[156:159]\n"
"ds_read_b64_tr_b16 a[218:219], v27 offset:4096\n"
"v_mfma_f32_16x16x32_bf16 v[172:175], a[204:207], a[136:139], v[172:175]\n"
"v_exp_f32 v55, -v55\n"
"v_mfma_f32_16x16x32_bf16 v[188:191], a[204:207], a[140:143], v[188:191]\n"
"ds_read_b64_tr_b16 a[220:221], v24 offset:4096\n"
"v_fmac_f32 v63, v63, v55\n"
"v_mfma_f32_16x16x32_bf16 v[204:207], a[204:207], a[144:147], v[204:207]\n"
"ds_read_b64_tr_b16 a[222:223], v28 offset:4096\n"
"v_mfma_f32_16x16x32_bf16 v[220:223], a[204:207], a[148:151], v[220:223]\n"
"v_rcp_f32 v63, v63\n"
"v_mfma_f32_16x16x32_bf16 v[236:239], a[204:207], a[152:155], v[236:239]\n"
"s_add_u32 m0, s62, 4096\n"
"s_add_u32 s61, s51, s53\n"
"v_mfma_f32_16x16x32_bf16 v[252:255], a[204:207], a[156:159], v[252:255]\n"
"s_waitcnt lgkmcnt(4)\n"
"v_fma_f32 v59, -v59, v63, v63\n"
"v_mfma_f32_16x16x32_bf16 v[128:131], a[208:211], a[160:163], v[128:131]\n"
"ds_read_b128 a[128:131], v17 offset:0\n"
"v_bfi_b32 v59, s70, v59, v51\n"
"v_mfma_f32_16x16x32_bf16 v[144:147], a[208:211], a[164:167], v[144:147]\n"
"ds_read_b128 a[132:135], v17 offset:2048\n"
"v_cvt_pk_bf16_f32 v127, v58, v59\n"
"v_mfma_f32_16x16x32_bf16 v[160:163], a[208:211], a[168:171], v[160:163]\n"
"v_mfma_f32_16x16x32_bf16 v[176:179], a[208:211], a[172:175], v[176:179]\n"
"ds_read_b128 a[136:139], v17 offset:4096\n"
"v_mfma_f32_16x16x32_bf16 v[192:195], a[208:211], a[176:179], v[192:195]\n"
"ds_read_b128 a[140:143], v17 offset:6144\n"
"v_mfma_f32_16x16x32_bf16 v[208:211], a[208:211], a[180:183], v[208:211]\n"
"s_waitcnt lgkmcnt(4)\n"
"v_mfma_f32_16x16x32_bf16 v[224:227], a[208:211], a[184:187], v[224:227]\n"
"buffer_load_dwordx4 v29, s[44:47], s61 offen offset:0 lds\n"
"v_mfma_f32_16x16x32_bf16 v[240:243], a[208:211], a[188:191], v[240:243]\n"
"ds_read_b128 a[144:147], v17 offset:8192\n"
"v_mfma_f32_16x16x32_bf16 v[132:135], a[212:215], a[160:163], v[132:135]\n"
"v_mfma_f32_16x16x32_bf16 v[148:151], a[212:215], a[164:167], v[148:151]\n"
"buffer_load_dwordx4 v30, s[44:47], s61 offen offset:1024 lds\n"
"v_mfma_f32_16x16x32_bf16 v[164:167], a[212:215], a[168:171], v[164:167]\n"
"ds_read_b128 a[148:151], v17 offset:10240\n"
"v_mfma_f32_16x16x32_bf16 v[180:183], a[212:215], a[172:175], v[180:183]\n"
"v_mfma_f32_16x16x32_bf16 v[196:199], a[212:215], a[176:179], v[196:199]\n"
"buffer_load_dwordx4 v31, s[44:47], s61 offen offset:2048 lds\n"
"v_mfma_f32_16x16x32_bf16 v[212:215], a[212:215], a[180:183], v[212:215]\n"
"ds_read_b128 a[152:155], v17 offset:12288\n"
"v_mfma_f32_16x16x32_bf16 v[228:231], a[212:215], a[184:187], v[228:231]\n"
"v_mfma_f32_16x16x32_bf16 v[244:247], a[212:215], a[188:191], v[244:247]\n"
"buffer_load_dwordx4 v32, s[44:47], s61 offen offset:3072 lds\n"
"v_mfma_f32_16x16x32_bf16 v[136:139], a[216:219], a[160:163], v[136:139]\n"
"ds_read_b128 a[156:159], v17 offset:14336\n"
"v_mfma_f32_16x16x32_bf16 v[152:155], a[216:219], a[164:167], v[152:155]\n"
"s_waitcnt vmcnt(20)\n"
"v_mfma_f32_16x16x32_bf16 v[168:171], a[216:219], a[168:171], v[168:171]\n"
"ds_read_b64_tr_b16 a[192:193], v21 offset:8192\n"
"v_mfma_f32_16x16x32_bf16 v[184:187], a[216:219], a[172:175], v[184:187]\n"
"ds_read_b64_tr_b16 a[194:195], v25 offset:8192\n"
"v_mfma_f32_16x16x32_bf16 v[200:203], a[216:219], a[176:179], v[200:203]\n"
"v_mfma_f32_16x16x32_bf16 v[216:219], a[216:219], a[180:183], v[216:219]\n"
"ds_read_b64_tr_b16 a[196:197], v22 offset:8192\n"
"v_mfma_f32_16x16x32_bf16 v[232:235], a[216:219], a[184:187], v[232:235]\n"
"ds_read_b64_tr_b16 a[198:199], v26 offset:8192\n"
"v_mfma_f32_16x16x32_bf16 v[248:251], a[216:219], a[188:191], v[248:251]\n"
"v_mfma_f32_16x16x32_bf16 v[140:143], a[220:223], a[160:163], v[140:143]\n"
"ds_read_b64_tr_b16 a[200:201], v23 offset:8192\n"
"v_mfma_f32_16x16x32_bf16 v[156:159], a[220:223], a[164:167], v[156:159]\n"
"ds_read_b64_tr_b16 a[202:203], v27 offset:8192\n"
"v_mfma_f32_16x16x32_bf16 v[172:175], a[220:223], a[168:171], v[172:175]\n"
"v_mfma_f32_16x16x32_bf16 v[188:191], a[220:223], a[172:175], v[188:191]\n"
"ds_read_b64_tr_b16 a[204:205], v24 offset:8192\n"
"v_mfma_f32_16x16x32_bf16 v[204:207], a[220:223], a[176:179], v[204:207]\n"
"ds_read_b64_tr_b16 a[206:207], v28 offset:8192\n"
"v_mfma_f32_16x16x32_bf16 v[220:223], a[220:223], a[180:183], v[220:223]\n"
"v_mfma_f32_16x16x32_bf16 v[236:239], a[220:223], a[184:187], v[236:239]\n"
"s_add_u32 m0, s62, 8192\n"
"s_add_u32 s61, s61, 0x8000\n"
"v_mfma_f32_16x16x32_bf16 v[252:255], a[220:223], a[188:191], v[252:255]\n"
"s_waitcnt lgkmcnt(4)\n"
"v_mfma_f32_16x16x32_bf16 v[128:131], a[192:195], a[128:131], v[128:131]\n"
"ds_read_b128 a[160:163], v17 offset:512\n"
"v_mfma_f32_16x16x32_bf16 v[144:147], a[192:195], a[132:135], v[144:147]\n"
"ds_read_b128 a[164:167], v17 offset:2560\n"
"v_mfma_f32_16x16x32_bf16 v[160:163], a[192:195], a[136:139], v[160:163]\n"
"v_mfma_f32_16x16x32_bf16 v[176:179], a[192:195], a[140:143], v[176:179]\n"
"ds_read_b128 a[168:171], v17 offset:4608\n"
"v_mfma_f32_16x16x32_bf16 v[192:195], a[192:195], a[144:147], v[192:195]\n"
"ds_read_b128 a[172:175], v17 offset:6656\n"
"v_mfma_f32_16x16x32_bf16 v[208:211], a[192:195], a[148:151], v[208:211]\n"
"s_waitcnt lgkmcnt(4)\n"
"v_mfma_f32_16x16x32_bf16 v[224:227], a[192:195], a[152:155], v[224:227]\n"
"buffer_load_dwordx4 v29, s[44:47], s61 offen offset:0 lds\n"
"v_mfma_f32_16x16x32_bf16 v[240:243], a[192:195], a[156:159], v[240:243]\n"
"ds_read_b128 a[176:179], v17 offset:8704\n"
"v_mfma_f32_16x16x32_bf16 v[132:135], a[196:199], a[128:131], v[132:135]\n"
"v_mfma_f32_16x16x32_bf16 v[148:151], a[196:199], a[132:135], v[148:151]\n"
"buffer_load_dwordx4 v30, s[44:47], s61 offen offset:1024 lds\n"
"v_mfma_f32_16x16x32_bf16 v[164:167], a[196:199], a[136:139], v[164:167]\n"
"ds_read_b128 a[180:183], v17 offset:10752\n"
"v_mfma_f32_16x16x32_bf16 v[180:183], a[196:199], a[140:143], v[180:183]\n"
"v_mfma_f32_16x16x32_bf16 v[196:199], a[196:199], a[144:147], v[196:199]\n"
"buffer_load_dwordx4 v31, s[44:47], s61 offen offset:2048 lds\n"
"v_mfma_f32_16x16x32_bf16 v[212:215], a[196:199], a[148:151], v[212:215]\n"
"ds_read_b128 a[184:187], v17 offset:12800\n"
"v_mfma_f32_16x16x32_bf16 v[228:231], a[196:199], a[152:155], v[228:231]\n"
"v_mfma_f32_16x16x32_bf16 v[244:247], a[196:199], a[156:159], v[244:247]\n"
"buffer_load_dwordx4 v32, s[44:47], s61 offen offset:3072 lds\n"
"v_mfma_f32_16x16x32_bf16 v[136:139], a[200:203], a[128:131], v[136:139]\n"
"ds_read_b128 a[188:191], v17 offset:14848\n"
"v_mfma_f32_16x16x32_bf16 v[152:155], a[200:203], a[132:135], v[152:155]\n"
"s_waitcnt vmcnt(12)\n"
"v_mfma_f32_16x16x32_bf16 v[168:171], a[200:203], a[136:139], v[168:171]\n"
"ds_read_b64_tr_b16 a[208:209], v21 offset:12288\n"
"v_add_u32 v17, 17408, v17\n"
"v_mfma_f32_16x16x32_bf16 v[184:187], a[200:203], a[140:143], v[184:187]\n"
"ds_read_b64_tr_b16 a[210:211], v25 offset:12288\n"
"v_mfma_f32_16x16x32_bf16 v[200:203], a[200:203], a[144:147], v[200:203]\n"
"v_mfma_f32_16x16x32_bf16 v[216:219], a[200:203], a[148:151], v[216:219]\n"
"ds_read_b64_tr_b16 a[212:213], v22 offset:12288\n"
"v_mfma_f32_16x16x32_bf16 v[232:235], a[200:203], a[152:155], v[232:235]\n"
"ds_read_b64_tr_b16 a[214:215], v26 offset:12288\n"
"v_mfma_f32_16x16x32_bf16 v[248:251], a[200:203], a[156:159], v[248:251]\n"
"v_mfma_f32_16x16x32_bf16 v[140:143], a[204:207], a[128:131], v[140:143]\n"
"ds_read_b64_tr_b16 a[216:217], v23 offset:12288\n"
"v_mfma_f32_16x16x32_bf16 v[156:159], a[204:207], a[132:135], v[156:159]\n"
"ds_read_b64_tr_b16 a[218:219], v27 offset:12288\n"
"v_mfma_f32_16x16x32_bf16 v[172:175], a[204:207], a[136:139], v[172:175]\n"
"v_mfma_f32_16x16x32_bf16 v[188:191], a[204:207], a[140:143], v[188:191]\n"
"ds_read_b64_tr_b16 a[220:221], v24 offset:12288\n"
"v_mfma_f32_16x16x32_bf16 v[204:207], a[204:207], a[144:147], v[204:207]\n"
"ds_read_b64_tr_b16 a[222:223], v28 offset:12288\n"
"v_mfma_f32_16x16x32_bf16 v[220:223], a[204:207], a[148:151], v[220:223]\n"
"v_mfma_f32_16x16x32_bf16 v[236:239], a[204:207], a[152:155], v[236:239]\n"
"s_add_u32 m0, s62, 12288\n"
"s_add_u32 s61, s52, s53\n"
"v_mfma_f32_16x16x32_bf16 v[252:255], a[204:207], a[156:159], v[252:255]\n"
"s_waitcnt lgkmcnt(4)\n"
"v_mfma_f32_16x16x32_bf16 v[128:131], a[208:211], a[160:163], v[128:131]\n"
"ds_read_b128 a[128:131], v18 offset:0\n"
"v_mfma_f32_16x16x32_bf16 v[144:147], a[208:211], a[164:167], v[144:147]\n"
"ds_read_b128 a[132:135], v18 offset:2048\n"
"v_mfma_f32_16x16x32_bf16 v[160:163], a[208:211], a[168:171], v[160:163]\n"
"ds_read_b64_tr_b16 a[80:81], v37 offset:0\n"
"ds_read_b64_tr_b16 a[82:83], v39 offset:0\n"
"v_mfma_f32_16x16x32_bf16 v[176:179], a[208:211], a[172:175], v[176:179]\n"
"ds_read_b128 a[136:139], v18 offset:4096\n"
"v_mfma_f32_16x16x32_bf16 v[192:195], a[208:211], a[176:179], v[192:195]\n"
"ds_read_b128 a[140:143], v18 offset:6144\n"
"v_mfma_f32_16x16x32_bf16 v[208:211], a[208:211], a[180:183], v[208:211]\n"
"s_waitcnt lgkmcnt(6)\n"
"v_mfma_f32_16x16x32_bf16 v[224:227], a[208:211], a[184:187], v[224:227]\n"
"buffer_load_dwordx4 v29, s[44:47], s61 offen offset:0 lds\n"
"v_mfma_f32_16x16x32_bf16 v[240:243], a[208:211], a[188:191], v[240:243]\n"
"ds_read_b128 a[144:147], v18 offset:8192\n"
"v_mfma_f32_16x16x32_bf16 v[132:135], a[212:215], a[160:163], v[132:135]\n"
"ds_read_b64_tr_b16 a[84:85], v38 offset:0\n"
"ds_read_b64_tr_b16 a[86:87], v40 offset:0\n"
"v_mfma_f32_16x16x32_bf16 v[148:151], a[212:215], a[164:167], v[148:151]\n"
"buffer_load_dwordx4 v30, s[44:47], s61 offen offset:1024 lds\n"
"v_mfma_f32_16x16x32_bf16 v[164:167], a[212:215], a[168:171], v[164:167]\n"
"ds_read_b128 a[148:151], v18 offset:10240\n"
"v_mfma_f32_16x16x32_bf16 v[180:183], a[212:215], a[172:175], v[180:183]\n"
"ds_read_b64_tr_b16 a[88:89], v37 offset:512\n"
"ds_read_b64_tr_b16 a[90:91], v39 offset:512\n"
"v_mfma_f32_16x16x32_bf16 v[196:199], a[212:215], a[176:179], v[196:199]\n"
"buffer_load_dwordx4 v31, s[44:47], s61 offen offset:2048 lds\n"
"v_mfma_f32_16x16x32_bf16 v[212:215], a[212:215], a[180:183], v[212:215]\n"
"ds_read_b128 a[152:155], v18 offset:12288\n"
"v_mfma_f32_16x16x32_bf16 v[228:231], a[212:215], a[184:187], v[228:231]\n"
"ds_read_b64_tr_b16 a[92:93], v38 offset:512\n"
"ds_read_b64_tr_b16 a[94:95], v40 offset:512\n"
"v_mfma_f32_16x16x32_bf16 v[244:247], a[212:215], a[188:191], v[244:247]\n"
"buffer_load_dwordx4 v32, s[44:47], s61 offen offset:3072 lds\n"
"s_xor_b32 s99, s98, 65536\n"
"v_mfma_f32_16x16x32_bf16 v[136:139], a[216:219], a[160:163], v[136:139]\n"
"ds_read_b128 a[156:159], v18 offset:14336\n"
"s_add_u32 s99, s99, 32768\n"
"v_mfma_f32_16x16x32_bf16 v[152:155], a[216:219], a[164:167], v[152:155]\n"
"s_waitcnt vmcnt(20)\n"
"v_mfma_f32_16x16x32_bf16 v[168:171], a[216:219], a[168:171], v[168:171]\n"
"ds_read_b64_tr_b16 a[192:193], v21 offset:16384\n"
"s_lshl_b32 m0, %[sm0], 2\n"
"v_mfma_f32_16x16x32_bf16 v[184:187], a[216:219], a[172:175], v[184:187]\n"
"ds_read_b64_tr_b16 a[194:195], v25 offset:16384\n"
"s_waitcnt lgkmcnt(3)\n"
"v_mfma_f32_16x16x32_bf16 v[200:203], a[216:219], a[176:179], v[200:203]\n"
"buffer_load_dwordx4 v33, s[64:67], s99 offen offset:0 lds\n"
"s_add_u32 s53, s53, 0x10000\n"
"s_and_b32 s53, s53, 0x3ffff\n"
"v_mfma_f32_16x16x32_bf16 v[216:219], a[216:219], a[180:183], v[216:219]\n"
"ds_read_b64_tr_b16 a[196:197], v22 offset:16384\n"
"v_mfma_f32_16x16x32_bf16 v[232:235], a[216:219], a[184:187], v[232:235]\n"
"ds_read_b64_tr_b16 a[198:199], v26 offset:16384\n"
"v_mfma_f32_16x16x32_bf16 v[248:251], a[216:219], a[188:191], v[248:251]\n"
"buffer_load_dwordx4 v34, s[64:67], s99 offen offset:1024 lds\n"
"v_mfma_f32_16x16x32_bf16 v[140:143], a[220:223], a[160:163], v[140:143]\n"
"ds_read_b64_tr_b16 a[200:201], v23 offset:16384\n"
"v_mfma_f32_16x16x32_bf16 v[156:159], a[220:223], a[164:167], v[156:159]\n"
"ds_read_b64_tr_b16 a[202:203], v27 offset:16384\n"
"v_mfma_f32_16x16x32_bf16 v[172:175], a[220:223], a[168:171], v[172:175]\n"
"buffer_load_dwordx4 v35, s[64:67], s99 offen offset:2048 lds\n"
"v_mfma_f32_16x16x32_bf16 v[188:191], a[220:223], a[172:175], v[188:191]\n"
"ds_read_b64_tr_b16 a[204:205], v24 offset:16384\n"
"v_mfma_f32_16x16x32_bf16 v[204:207], a[220:223], a[176:179], v[204:207]\n"
"ds_read_b64_tr_b16 a[206:207], v28 offset:16384\n"
"v_mfma_f32_16x16x32_bf16 v[220:223], a[220:223], a[180:183], v[220:223]\n"
"buffer_load_dwordx4 v36, s[64:67], s99 offen offset:3072 lds\n"
"v_mfma_f32_16x16x32_bf16 v[236:239], a[220:223], a[184:187], v[236:239]\n"
"s_add_u32 m0, s62, 16384\n"
"s_add_u32 s61, s61, 0x8000\n"
"v_mfma_f32_16x16x32_bf16 v[252:255], a[220:223], a[188:191], v[252:255]\n"
"s_waitcnt lgkmcnt(4)\n"
"v_mfma_f32_16x16x32_bf16 v[128:131], a[192:195], a[128:131], v[128:131]\n"
"ds_read_b128 a[160:163], v18 offset:512\n"
"v_mfma_f32_16x16x32_bf16 v[144:147], a[192:195], a[132:135], v[144:147]\n"
"ds_read_b128 a[164:167], v18 offset:2560\n"
"v_mfma_f32_16x16x32_bf16 v[160:163], a[192:195], a[136:139], v[160:163]\n"
"v_mfma_f32_16x16x32_bf16 v[176:179], a[192:195], a[140:143], v[176:179]\n"
"ds_read_b128 a[168:171], v18 offset:4608\n"
"v_mfma_f32_16x16x32_bf16 v[192:195], a[192:195], a[144:147], v[192:195]\n"
"ds_read_b128 a[172:175], v18 offset:6656\n"
"v_mfma_f32_16x16x32_bf16 v[208:211], a[192:195], a[148:151], v[208:211]\n"
"s_waitcnt lgkmcnt(4)\n"
"v_mfma_f32_16x16x32_bf16 v[224:227], a[192:195], a[152:155], v[224:227]\n"
"buffer_load_dwordx4 v29, s[44:47], s61 offen offset:0 lds\n"
"v_mfma_f32_16x16x32_bf16 v[240:243], a[192:195], a[156:159], v[240:243]\n"
"ds_read_b128 a[176:179], v18 offset:8704\n"
"v_mfma_f32_16x16x32_bf16 v[132:135], a[196:199], a[128:131], v[132:135]\n"
"v_mfma_f32_16x16x32_bf16 v[148:151], a[196:199], a[132:135], v[148:151]\n"
"buffer_load_dwordx4 v30, s[44:47], s61 offen offset:1024 lds\n"
"v_mfma_f32_16x16x32_bf16 v[164:167], a[196:199], a[136:139], v[164:167]\n"
"ds_read_b128 a[180:183], v18 offset:10752\n"
"v_mfma_f32_16x16x32_bf16 v[180:183], a[196:199], a[140:143], v[180:183]\n"
"v_mfma_f32_16x16x32_bf16 v[196:199], a[196:199], a[144:147], v[196:199]\n"
"buffer_load_dwordx4 v31, s[44:47], s61 offen offset:2048 lds\n"
"v_mfma_f32_16x16x32_bf16 v[212:215], a[196:199], a[148:151], v[212:215]\n"
"ds_read_b128 a[184:187], v18 offset:12800\n"
"v_mfma_f32_16x16x32_bf16 v[228:231], a[196:199], a[152:155], v[228:231]\n"
"v_mfma_f32_16x16x32_bf16 v[244:247], a[196:199], a[156:159], v[244:247]\n"
"buffer_load_dwordx4 v32, s[44:47], s61 offen offset:3072 lds\n"
"v_mfma_f32_16x16x32_bf16 v[136:139], a[200:203], a[128:131], v[136:139]\n"
"ds_read_b128 a[188:191], v18 offset:14848\n"
"v_mfma_f32_16x16x32_bf16 v[152:155], a[200:203], a[132:135], v[152:155]\n"
"s_waitcnt vmcnt(20)\n"
"v_mfma_f32_16x16x32_bf16 v[168:171], a[200:203], a[136:139], v[168:171]\n"
"ds_read_b64_tr_b16 a[208:209], v21 offset:0\n"
"v_add_u32 v18, 17408, v18\n"
"v_mfma_f32_16x16x32_bf16 v[184:187], a[200:203], a[140:143], v[184:187]\n"
"ds_read_b64_tr_b16 a[210:211], v25 offset:0\n"
"v_mfma_f32_16x16x32_bf16 v[200:203], a[200:203], a[144:147], v[200:203]\n"
"v_mfma_f32_16x16x32_bf16 v[216:219], a[200:203], a[148:151], v[216:219]\n"
"ds_read_b64_tr_b16 a[212:213], v22 offset:0\n"
"v_mfma_f32_16x16x32_bf16 v[232:235], a[200:203], a[152:155], v[232:235]\n"
"ds_read_b64_tr_b16 a[214:215], v26 offset:0\n"
"v_mfma_f32_16x16x32_bf16 v[248:251], a[200:203], a[156:159], v[248:251]\n"
"v_mfma_f32_16x16x32_bf16 v[140:143], a[204:207], a[128:131], v[140:143]\n"
"ds_read_b64_tr_b16 a[216:217], v23 offset:0\n"
"v_mfma_f32_16x16x32_bf16 v[156:159], a[204:207], a[132:135], v[156:159]\n"
"ds_read_b64_tr_b16 a[218:219], v27 offset:0\n"
"v_mfma_f32_16x16x32_bf16 v[172:175], a[204:207], a[136:139], v[172:175]\n"
"v_mfma_f32_16x16x32_bf16 v[188:191], a[204:207], a[140:143], v[188:191]\n"
"ds_read_b64_tr_b16 a[220:221], v24 offset:0\n"
"v_mfma_f32_16x16x32_bf16 v[204:207], a[204:207], a[144:147], v[204:207]\n"
"ds_read_b64_tr_b16 a[222:223], v28 offset:0\n"
"v_mfma_f32_16x16x32_bf16 v[220:223], a[204:207], a[148:151], v[220:223]\n"
"s_xor_b32 s99, s98, 65536\n"
"v_mfma_f32_16x16x32_bf16 v[236:239], a[204:207], a[152:155], v[236:239]\n"
"s_mov_b32 m0, s62\n"
"s_add_u32 s99, s99, 49152\n"
"v_mfma_f32_16x16x32_bf16 v[252:255], a[204:207], a[156:159], v[252:255]\n"
"s_waitcnt lgkmcnt(4)\n"
"v_mfma_f32_16x16x32_bf16 v[128:131], a[208:211], a[160:163], v[128:131]\n"
"ds_read_b128 a[128:131], v19 offset:0\n"
"v_mfma_f32_16x16x32_bf16 v[144:147], a[208:211], a[164:167], v[144:147]\n"
"ds_read_b128 a[132:135], v19 offset:2048\n"
"v_mfma_f32_16x16x32_bf16 v[160:163], a[208:211], a[168:171], v[160:163]\n"
"v_mfma_f32_16x16x32_bf16 v[176:179], a[208:211], a[172:175], v[176:179]\n"
"ds_read_b128 a[136:139], v19 offset:4096\n"
"v_mfma_f32_16x16x32_bf16 v[192:195], a[208:211], a[176:179], v[192:195]\n"
"ds_read_b128 a[140:143], v19 offset:6144\n"
"v_mfma_f32_16x16x32_bf16 v[208:211], a[208:211], a[180:183], v[208:211]\n"
"s_waitcnt lgkmcnt(4)\n"
"v_mfma_f32_16x16x32_bf16 v[224:227], a[208:211], a[184:187], v[224:227]\n"
"buffer_load_dwordx4 v33, s[64:67], s99 offen offset:0 lds\n"
"v_mfma_f32_16x16x32_bf16 v[240:243], a[208:211], a[188:191], v[240:243]\n"
"ds_read_b128 a[144:147], v19 offset:8192\n"
"v_mfma_f32_16x16x32_bf16 v[132:135], a[212:215], a[160:163], v[132:135]\n"
"v_mfma_f32_16x16x32_bf16 v[148:151], a[212:215], a[164:167], v[148:151]\n"
"buffer_load_dwordx4 v34, s[64:67], s99 offen offset:1024 lds\n"
"v_mfma_f32_16x16x32_bf16 v[164:167], a[212:215], a[168:171], v[164:167]\n"
"ds_read_b128 a[148:151], v19 offset:10240\n"
"v_mfma_f32_16x16x32_bf16 v[180:183], a[212:215], a[172:175], v[180:183]\n"
"v_mfma_f32_16x16x32_bf16 v[196:199], a[212:215], a[176:179], v[196:199]\n"
"buffer_load_dwordx4 v35, s[64:67], s99 offen offset:2048 lds\n"
"v_mfma_f32_16x16x32_bf16 v[212:215], a[212:215], a[180:183], v[212:215]\n"
"ds_read_b128 a[152:155], v19 offset:12288\n"
"v_mfma_f32_16x16x32_bf16 v[228:231], a[212:215], a[184:187], v[228:231]\n"
"v_mfma_f32_16x16x32_bf16 v[244:247], a[212:215], a[188:191], v[244:247]\n"
"buffer_load_dwordx4 v36, s[64:67], s99 offen offset:3072 lds\n"
"v_mfma_f32_16x16x32_bf16 v[136:139], a[216:219], a[160:163], v[136:139]\n"
"ds_read_b128 a[156:159], v19 offset:14336\n"
"v_mfma_f32_16x16x32_bf16 v[152:155], a[216:219], a[164:167], v[152:155]\n"
"s_waitcnt vmcnt(20)\n"
"v_mfma_f32_16x16x32_bf16 v[168:171], a[216:219], a[168:171], v[168:171]\n"
"ds_read_b64_tr_b16 a[192:193], v21 offset:4096\n"
"v_mfma_f32_16x16x32_bf16 v[184:187], a[216:219], a[172:175], v[184:187]\n"
"ds_read_b64_tr_b16 a[194:195], v25 offset:4096\n"
"v_mfma_f32_16x16x32_bf16 v[200:203], a[216:219], a[176:179], v[200:203]\n"
"v_mfma_f32_16x16x32_bf16 v[216:219], a[216:219], a[180:183], v[216:219]\n"
"ds_read_b64_tr_b16 a[196:197], v22 offset:4096\n"
"v_mfma_f32_16x16x32_bf16 v[232:235], a[216:219], a[184:187], v[232:235]\n"
"ds_read_b64_tr_b16 a[198:199], v26 offset:4096\n"
"v_mfma_f32_16x16x32_bf16 v[248:251], a[216:219], a[188:191], v[248:251]\n"
"v_mfma_f32_16x16x32_bf16 v[140:143], a[220:223], a[160:163], v[140:143]\n"
"ds_read_b64_tr_b16 a[200:201], v23 offset:4096\n"
"v_mfma_f32_16x16x32_bf16 v[156:159], a[220:223], a[164:167], v[156:159]\n"
"ds_read_b64_tr_b16 a[202:203], v27 offset:4096\n"
"v_mfma_f32_16x16x32_bf16 v[172:175], a[220:223], a[168:171], v[172:175]\n"
"v_mfma_f32_16x16x32_bf16 v[188:191], a[220:223], a[172:175], v[188:191]\n"
"ds_read_b64_tr_b16 a[204:205], v24 offset:4096\n"
"v_mfma_f32_16x16x32_bf16 v[204:207], a[220:223], a[176:179], v[204:207]\n"
"ds_read_b64_tr_b16 a[206:207], v28 offset:4096\n"
"v_mfma_f32_16x16x32_bf16 v[220:223], a[220:223], a[180:183], v[220:223]\n"
"v_mfma_f32_16x16x32_bf16 v[236:239], a[220:223], a[184:187], v[236:239]\n"
"v_mfma_f32_16x16x32_bf16 v[252:255], a[220:223], a[188:191], v[252:255]\n"
"s_waitcnt lgkmcnt(4)\n"
"v_mfma_f32_16x16x32_bf16 v[128:131], a[192:195], a[128:131], v[128:131]\n"
"ds_read_b128 a[160:163], v19 offset:512\n"
"v_mfma_f32_16x16x32_bf16 v[144:147], a[192:195], a[132:135], v[144:147]\n"
"ds_read_b128 a[164:167], v19 offset:2560\n"
"v_mfma_f32_16x16x32_bf16 v[160:163], a[192:195], a[136:139], v[160:163]\n"
"v_mfma_f32_16x16x32_bf16 v[176:179], a[192:195], a[140:143], v[176:179]\n"
"ds_read_b128 a[168:171], v19 offset:4608\n"
"v_mfma_f32_16x16x32_bf16 v[192:195], a[192:195], a[144:147], v[192:195]\n"
"ds_read_b128 a[172:175], v19 offset:6656\n"
"v_mfma_f32_16x16x32_bf16 v[208:211], a[192:195], a[148:151], v[208:211]\n"
"s_waitcnt lgkmcnt(4)\n"
"v_mfma_f32_16x16x32_bf16 v[224:227], a[192:195], a[152:155], v[224:227]\n"
"v_mfma_f32_16x16x32_bf16 v[240:243], a[192:195], a[156:159], v[240:243]\n"
"ds_read_b128 a[176:179], v19 offset:8704\n"
"v_mfma_f32_16x16x32_bf16 v[132:135], a[196:199], a[128:131], v[132:135]\n"
"v_mfma_f32_16x16x32_bf16 v[148:151], a[196:199], a[132:135], v[148:151]\n"
"v_mfma_f32_16x16x32_bf16 v[164:167], a[196:199], a[136:139], v[164:167]\n"
"ds_read_b128 a[180:183], v19 offset:10752\n"
"v_mfma_f32_16x16x32_bf16 v[180:183], a[196:199], a[140:143], v[180:183]\n"
"v_mfma_f32_16x16x32_bf16 v[196:199], a[196:199], a[144:147], v[196:199]\n"
"v_mfma_f32_16x16x32_bf16 v[212:215], a[196:199], a[148:151], v[212:215]\n"
"ds_read_b128 a[184:187], v19 offset:12800\n"
"v_mfma_f32_16x16x32_bf16 v[228:231], a[196:199], a[152:155], v[228:231]\n"
"v_mfma_f32_16x16x32_bf16 v[244:247], a[196:199], a[156:159], v[244:247]\n"
"v_mfma_f32_16x16x32_bf16 v[136:139], a[200:203], a[128:131], v[136:139]\n"
"ds_read_b128 a[188:191], v19 offset:14848\n"
"v_mfma_f32_16x16x32_bf16 v[152:155], a[200:203], a[132:135], v[152:155]\n"
"s_waitcnt vmcnt(8)\n"
"v_mfma_f32_16x16x32_bf16 v[168:171], a[200:203], a[136:139], v[168:171]\n"
"ds_read_b64_tr_b16 a[208:209], v21 offset:8192\n"
"v_add_u32 v19, 17408, v19\n"
"v_mfma_f32_16x16x32_bf16 v[184:187], a[200:203], a[140:143], v[184:187]\n"
"ds_read_b64_tr_b16 a[210:211], v25 offset:8192\n"
"v_mfma_f32_16x16x32_bf16 v[200:203], a[200:203], a[144:147], v[200:203]\n"
"v_mfma_f32_16x16x32_bf16 v[216:219], a[200:203], a[148:151], v[216:219]\n"
"ds_read_b64_tr_b16 a[212:213], v22 offset:8192\n"
"v_mfma_f32_16x16x32_bf16 v[232:235], a[200:203], a[152:155], v[232:235]\n"
"ds_read_b64_tr_b16 a[214:215], v26 offset:8192\n"
"v_mfma_f32_16x16x32_bf16 v[248:251], a[200:203], a[156:159], v[248:251]\n"
"v_mfma_f32_16x16x32_bf16 v[140:143], a[204:207], a[128:131], v[140:143]\n"
"ds_read_b64_tr_b16 a[216:217], v23 offset:8192\n"
"v_mfma_f32_16x16x32_bf16 v[156:159], a[204:207], a[132:135], v[156:159]\n"
"ds_read_b64_tr_b16 a[218:219], v27 offset:8192\n"
"v_mfma_f32_16x16x32_bf16 v[172:175], a[204:207], a[136:139], v[172:175]\n"
"v_mfma_f32_16x16x32_bf16 v[188:191], a[204:207], a[140:143], v[188:191]\n"
"ds_read_b64_tr_b16 a[220:221], v24 offset:8192\n"
"v_mfma_f32_16x16x32_bf16 v[204:207], a[204:207], a[144:147], v[204:207]\n"
"ds_read_b64_tr_b16 a[222:223], v28 offset:8192\n"
"v_mfma_f32_16x16x32_bf16 v[220:223], a[204:207], a[148:151], v[220:223]\n"
"v_mfma_f32_16x16x32_bf16 v[236:239], a[204:207], a[152:155], v[236:239]\n"
"v_mfma_f32_16x16x32_bf16 v[252:255], a[204:207], a[156:159], v[252:255]\n"
"s_waitcnt lgkmcnt(4)\n"
"v_mfma_f32_16x16x32_bf16 v[128:131], a[208:211], a[160:163], v[128:131]\n"
"ds_read_b128 a[128:131], v20 offset:0\n"
"v_mfma_f32_16x16x32_bf16 v[144:147], a[208:211], a[164:167], v[144:147]\n"
"ds_read_b128 a[132:135], v20 offset:2048\n"
"v_mfma_f32_16x16x32_bf16 v[160:163], a[208:211], a[168:171], v[160:163]\n"
"ds_read_b64_tr_b16 a[96:97], v37 offset:0\n"
"ds_read_b64_tr_b16 a[98:99], v39 offset:0\n"
"v_mfma_f32_16x16x32_bf16 v[176:179], a[208:211], a[172:175], v[176:179]\n"
"ds_read_b128 a[136:139], v20 offset:4096\n"
"v_mfma_f32_16x16x32_bf16 v[192:195], a[208:211], a[176:179], v[192:195]\n"
"ds_read_b128 a[140:143], v20 offset:6144\n"
"v_mfma_f32_16x16x32_bf16 v[208:211], a[208:211], a[180:183], v[208:211]\n"
"s_waitcnt lgkmcnt(6)\n"
"v_mfma_f32_16x16x32_bf16 v[224:227], a[208:211], a[184:187], v[224:227]\n"
"ds_write_b128 %[vZw], v[96:99] offset:4096\n"
"v_mfma_f32_16x16x32_bf16 v[240:243], a[208:211], a[188:191], v[240:243]\n"
"ds_read_b128 a[144:147], v20 offset:8192\n"
"v_mfma_f32_16x16x32_bf16 v[132:135], a[212:215], a[160:163], v[132:135]\n"
"ds_read_b64_tr_b16 a[100:101], v38 offset:0\n"
"ds_read_b64_tr_b16 a[102:103], v40 offset:0\n"
"v_mfma_f32_16x16x32_bf16 v[148:151], a[212:215], a[164:167], v[148:151]\n"
"ds_write_b128 %[vZw], v[100:103] offset:5120\n"
"v_mfma_f32_16x16x32_bf16 v[164:167], a[212:215], a[168:171], v[164:167]\n"
"ds_read_b128 a[148:151], v20 offset:10240\n"
"v_mfma_f32_16x16x32_bf16 v[180:183], a[212:215], a[172:175], v[180:183]\n"
"ds_read_b64_tr_b16 a[104:105], v37 offset:512\n"
"ds_read_b64_tr_b16 a[106:107], v39 offset:512\n"
"v_mfma_f32_16x16x32_bf16 v[196:199], a[212:215], a[176:179], v[196:199]\n"
"ds_write_b128 %[vZw], v[104:107] offset:6144\n"
"v_mfma_f32_16x16x32_bf16 v[212:215], a[212:215], a[180:183], v[212:215]\n"
"ds_read_b128 a[152:155], v20 offset:12288\n"
"v_mfma_f32_16x16x32_bf16 v[228:231], a[212:215], a[184:187], v[228:231]\n"
"ds_read_b64_tr_b16 a[108:109], v38 offset:512\n"
"ds_read_b64_tr_b16 a[110:111], v40 offset:512\n"
"v_mfma_f32_16x16x32_bf16 v[244:247], a[212:215], a[188:191], v[244:247]\n"
"ds_write_b128 %[vZw], v[108:111] offset:7168\n"
"v_mfma_f32_16x16x32_bf16 v[136:139], a[216:219], a[160:163], v[136:139]\n"
"ds_read_b128 a[156:159], v20 offset:14336\n"
"v_mfma_f32_16x16x32_bf16 v[152:155], a[216:219], a[164:167], v[152:155]\n"
"ds_write_b128 %[vZw], v[112:115] offset:8192\n"
"s_waitcnt vmcnt(12)\n"
"v_mfma_f32_16x16x32_bf16 v[168:171], a[216:219], a[168:171], v[168:171]\n"
"ds_read_b64_tr_b16 a[192:193], v21 offset:12288\n"
"v_mfma_f32_16x16x32_bf16 v[184:187], a[216:219], a[172:175], v[184:187]\n"
"ds_read_b64_tr_b16 a[194:195], v25 offset:12288\n"
"s_sub_u32 s99, s62, %[sm0]\n"
"v_mfma_f32_16x16x32_bf16 v[200:203], a[216:219], a[176:179], v[200:203]\n"
"ds_write_b128 %[vZw], v[116:119] offset:9216\n"
"s_mul_i32 s39, %[sm0], 3\n"
"s_sub_u32 s99, s99, s39\n"
"v_mfma_f32_16x16x32_bf16 v[216:219], a[216:219], a[180:183], v[216:219]\n"
"ds_read_b64_tr_b16 a[196:197], v22 offset:12288\n"
"v_add_u32 v37, s99, v37\n"
"v_mfma_f32_16x16x32_bf16 v[232:235], a[216:219], a[184:187], v[232:235]\n"
"ds_read_b64_tr_b16 a[198:199], v26 offset:12288\n"
"v_add_u32 v38, s99, v38\n"
"v_mfma_f32_16x16x32_bf16 v[248:251], a[216:219], a[188:191], v[248:251]\n"
"ds_write_b128 %[vZw], v[120:123] offset:10240\n"
"v_add_u32 v39, s99, v39\n"
"v_mfma_f32_16x16x32_bf16 v[140:143], a[220:223], a[160:163], v[140:143]\n"
"ds_read_b64_tr_b16 a[200:201], v23 offset:12288\n"
"v_add_u32 v40, s99, v40\n"
"v_mfma_f32_16x16x32_bf16 v[156:159], a[220:223], a[164:167], v[156:159]\n"
"ds_read_b64_tr_b16 a[202:203], v27 offset:12288\n"
"v_mfma_f32_16x16x32_bf16 v[172:175], a[220:223], a[168:171], v[172:175]\n"
"ds_write_b128 %[vZw], v[124:127] offset:11264\n"
"v_mfma_f32_16x16x32_bf16 v[188:191], a[220:223], a[172:175], v[188:191]\n"
"ds_read_b64_tr_b16 a[204:205], v24 offset:12288\n"
"v_mfma_f32_16x16x32_bf16 v[204:207], a[220:223], a[176:179], v[204:207]\n"
"ds_read_b64_tr_b16 a[206:207], v28 offset:12288\n"
"v_mfma_f32_16x16x32_bf16 v[220:223], a[220:223], a[180:183], v[220:223]\n"
"v_mfma_f32_16x16x32_bf16 v[236:239], a[220:223], a[184:187], v[236:239]\n"
"v_mfma_f32_16x16x32_bf16 v[252:255], a[220:223], a[188:191], v[252:255]\n"
"s_waitcnt lgkmcnt(6)\n"
"s_waitcnt vmcnt(4)\n"
"v_mfma_f32_16x16x32_bf16 v[128:131], a[192:195], a[128:131], v[128:131]\n"
"ds_read_b128 a[160:163], v20 offset:512\n"
"ds_read_b64_tr_b16 a[208:209], v21 offset:16384\n"
"v_mfma_f32_16x16x32_bf16 v[144:147], a[192:195], a[132:135], v[144:147]\n"
"ds_read_b64_tr_b16 a[210:211], v25 offset:16384\n"
"ds_read_b64_tr_b16 a[212:213], v22 offset:16384\n"
"v_mfma_f32_16x16x32_bf16 v[132:135], a[196:199], a[128:131], v[132:135]\n"
"ds_read_b64_tr_b16 a[214:215], v26 offset:16384\n"
"ds_read_b64_tr_b16 a[216:217], v23 offset:16384\n"
"v_mfma_f32_16x16x32_bf16 v[148:151], a[196:199], a[132:135], v[148:151]\n"
"s_waitcnt lgkmcnt(6)\n"
"ds_read_b64_tr_b16 a[218:219], v27 offset:16384\n"
"ds_read_b64_tr_b16 a[220:221], v24 offset:16384\n"
"v_mfma_f32_16x16x32_bf16 v[136:139], a[200:203], a[128:131], v[136:139]\n"
"ds_read_b64_tr_b16 a[222:223], v28 offset:16384\n"
"v_mfma_f32_16x16x32_bf16 v[152:155], a[200:203], a[132:135], v[152:155]\n"
"v_mfma_f32_16x16x32_bf16 v[140:143], a[204:207], a[128:131], v[140:143]\n"
"ds_read_b128 a[164:167], v20 offset:2560\n"
"v_mfma_f32_16x16x32_bf16 v[156:159], a[204:207], a[132:135], v[156:159]\n"
"v_mfma_f32_16x16x32_bf16 v[160:163], a[192:195], a[136:139], v[160:163]\n"
"v_mfma_f32_16x16x32_bf16 v[164:167], a[196:199], a[136:139], v[164:167]\n"
"s_waitcnt lgkmcnt(12)\n"
"s_barrier\n"
"v_add_u32 v112, 40960, %[vZr]\n"
"v_mfma_f32_16x16x32_bf16 v[168:171], a[200:203], a[136:139], v[168:171]\n"
"v_mfma_f32_16x16x32_bf16 v[172:175], a[204:207], a[136:139], v[172:175]\n"
"s_waitcnt lgkmcnt(1)\n"
"v_mfma_f32_16x16x32_bf16 v[128:131], a[208:211], a[160:163], v[128:131]\n"
"ds_read_b128 a[168:171], v20 offset:4608\n"
"v_mfma_f32_16x16x32_bf16 v[132:135], a[212:215], a[160:163], v[132:135]\n"
"v_mfma_f32_16x16x32_bf16 v[136:139], a[216:219], a[160:163], v[136:139]\n"
"ds_read_b128 a[224:227], %[vZr] offset:4096\n"
"v_mfma_f32_16x16x32_bf16 v[140:143], a[220:223], a[160:163], v[140:143]\n"
"v_mfma_f32_16x16x32_bf16 v[176:179], a[192:195], a[140:143], v[176:179]\n"
"v_mfma_f32_16x16x32_bf16 v[180:183], a[196:199], a[140:143], v[180:183]\n"
"v_mfma_f32_16x16x32_bf16 v[184:187], a[200:203], a[140:143], v[184:187]\n"
"ds_read_b128 a[172:175], v20 offset:6656\n"
"v_mul_f32 v128, s68, v128\n"
"v_mul_f32 v129, s68, v129\n"
"v_mul_f32 v130, s68, v130\n"
"v_mfma_f32_16x16x32_bf16 v[188:191], a[204:207], a[140:143], v[188:191]\n"
"s_waitcnt lgkmcnt(3)\n"
"v_mul_f32 v131, s68, v131\n"
"v_mul_f32 v132, s68, v132\n"
"v_mfma_f32_16x16x32_bf16 v[144:147], a[208:211], a[164:167], v[144:147]\n"
"v_mul_f32 v133, s68, v133\n"
"v_mul_f32 v134, s68, v134\n"
"v_mul_f32 v135, s68, v135\n"
"v_mfma_f32_16x16x32_bf16 v[148:151], a[212:215], a[164:167], v[148:151]\n"
"ds_read_b128 a[228:231], %[vZr] offset:4608\n"
"v_mul_f32 v136, s69, v136\n"
"v_mul_f32 v137, s69, v137\n"
"v_mfma_f32_16x16x32_bf16 v[152:155], a[216:219], a[164:167], v[152:155]\n"
"v_mul_f32 v138, s69, v138\n"
"v_mul_f32 v139, s69, v139\n"
"v_mfma_f32_16x16x32_bf16 v[156:159], a[220:223], a[164:167], v[156:159]\n"
"v_mul_f32 v140, s69, v140\n"
"v_mul_f32 v141, s69, v141\n"
"v_mul_f32 v142, s69, v142\n"
"v_mfma_f32_16x16x32_bf16 v[192:195], a[192:195], a[144:147], v[192:195]\n"
"ds_read_b128 a[176:179], v20 offset:8704\n"
"v_mul_f32 v143, s69, v143\n"
"v_exp_f32 v48, -|v128|\n"
"v_mfma_f32_16x16x32_bf16 v[196:199], a[196:199], a[144:147], v[196:199]\n"
"v_exp_f32 v49, -|v129|\n"
"v_mfma_f32_16x16x32_bf16 v[200:203], a[200:203], a[144:147], v[200:203]\n"
"v_exp_f32 v50, -|v130|\n"
"v_mfma_f32_16x16x32_bf16 v[204:207], a[204:207], a[144:147], v[204:207]\n"
"s_waitcnt lgkmcnt(4)\n"
"ds_read_b128 a[232:235], %[vZr] offset:24576\n"
"v_exp_f32 v51, -|v131|\n"
"v_mfma_f32_16x16x32_bf16 v[160:163], a[208:211], a[168:171], v[160:163]\n"
"v_exp_f32 v52, -|v132|\n"
"v_exp_f32 v53, -|v133|\n"
"v_mfma_f32_16x16x32_bf16 v[164:167], a[212:215], a[168:171], v[164:167]\n"
"v_exp_f32 v54, -|v134|\n"
"v_mfma_f32_16x16x32_bf16 v[168:171], a[216:219], a[168:171], v[168:171]\n"
"ds_read_b128 a[180:183], v20 offset:10752\n"
"v_exp_f32 v55, -|v135|\n"
"v_mfma_f32_16x16x32_bf16 v[172:175], a[220:223], a[168:171], v[172:175]\n"
"v_exp_f32 v136, -v136\n"
"v_mfma_f32_16x16x32_bf16 v[208:211], a[192:195], a[148:151], v[208:211]\n"
"v_exp_f32 v137, -v137\n"
"v_mfma_f32_16x16x32_bf16 v[212:215], a[196:199], a[148:151], v[212:215]\n"
"ds_read_b128 a[236:239], %[vZr] offset:25088\n"
"v_exp_f32 v138, -v138\n"
"v_exp_f32 v139, -v139\n"
"v_mfma_f32_16x16x32_bf16 v[216:219], a[200:203], a[148:151], v[216:219]\n"
"v_exp_f32 v140, -v140\n"
"v_mfma_f32_16x16x32_bf16 v[220:223], a[204:207], a[148:151], v[220:223]\n"
"s_waitcnt lgkmcnt(5)\n"
"v_exp_f32 v141, -v141\n"
"v_mfma_f32_16x16x32_bf16 v[176:179], a[208:211], a[172:175], v[176:179]\n"
"ds_read_b128 a[184:187], v20 offset:12800\n"
"v_exp_f32 v142, -v142\n"
"v_mfma_f32_16x16x32_bf16 v[180:183], a[212:215], a[172:175], v[180:183]\n"
"v_exp_f32 v143, -v143\n"
"v_mfma_f32_16x16x32_bf16 v[184:187], a[216:219], a[172:175], v[184:187]\n"
"v_add_f32 v56, 1.0, v48\n"
"v_add_f32 v57, 1.0, v49\n"
"v_mfma_f32_16x16x32_bf16 v[188:191], a[220:223], a[172:175], v[188:191]\n"
"ds_read_b128 a[240:243], v112 offset:4096\n"
"v_add_f32 v58, 1.0, v50\n"
"v_add_f32 v59, 1.0, v51\n"
"v_add_f32 v60, 1.0, v52\n"
"v_mfma_f32_16x16x32_bf16 v[224:227], a[192:195], a[152:155], v[224:227]\n"
"v_add_f32 v61, 1.0, v53\n"
"v_add_f32 v62, 1.0, v54\n"
"v_mfma_f32_16x16x32_bf16 v[228:231], a[196:199], a[152:155], v[228:231]\n"
"v_add_f32 v63, 1.0, v55\n"
"v_fmac_f32 v56, v56, v136\n"
"v_fmac_f32 v57, v57, v137\n"
"v_mfma_f32_16x16x32_bf16 v[232:235], a[200:203], a[152:155], v[232:235]\n"
"ds_read_b128 a[188:191], v20 offset:14848\n"
"v_fmac_f32 v58, v58, v138\n"
"v_fmac_f32 v59, v59, v139\n"
"v_mfma_f32_16x16x32_bf16 v[236:239], a[204:207], a[152:155], v[236:239]\n"
"s_waitcnt lgkmcnt(6)\n"
"v_fmac_f32 v60, v60, v140\n"
"v_fmac_f32 v61, v61, v141\n"
"v_mfma_f32_16x16x32_bf16 v[192:195], a[208:211], a[176:179], v[192:195]\n"
"v_fmac_f32 v62, v62, v142\n"
"v_fmac_f32 v63, v63, v143\n"
"v_rcp_f32 v56, v56\n"
"v_mfma_f32_16x16x32_bf16 v[196:199], a[212:215], a[176:179], v[196:199]\n"
"ds_read_b128 a[244:247], v112 offset:4608\n"
"v_rcp_f32 v57, v57\n"
"v_mfma_f32_16x16x32_bf16 v[200:203], a[216:219], a[176:179], v[200:203]\n"
"v_rcp_f32 v58, v58\n"
"v_mfma_f32_16x16x32_bf16 v[204:207], a[220:223], a[176:179], v[204:207]\n"
"v_rcp_f32 v59, v59\n"
"v_mfma_f32_16x16x32_bf16 v[240:243], a[192:195], a[156:159], v[240:243]\n"
"ds_read_b128 a[248:251], v112 offset:24576\n"
"v_rcp_f32 v60, v60\n"
"v_mfma_f32_16x16x32_bf16 v[244:247], a[196:199], a[156:159], v[244:247]\n"
"v_rcp_f32 v61, v61\n"
"v_rcp_f32 v62, v62\n"
"v_mfma_f32_16x16x32_bf16 v[248:251], a[200:203], a[156:159], v[248:251]\n"
"ds_read_b128 a[252:255], v112 offset:25088\n"
"v_rcp_f32 v63, v63\n"
"v_mfma_f32_16x16x32_bf16 v[252:255], a[204:207], a[156:159], v[252:255]\n"
"s_waitcnt lgkmcnt(7)\n"
"v_fma_f32 v48, -v48, v56, v56\n"
"v_mfma_f32_16x16x32_bf16 v[208:211], a[208:211], a[180:183], v[208:211]\n"
"s_waitcnt vmcnt(0)\n"
"v_fma_f32 v49, -v49, v57, v57\n"
"v_fma_f32 v50, -v50, v58, v58\n"
"v_fma_f32 v51, -v51, v59, v59\n"
"v_mfma_f32_16x16x32_bf16 v[212:215], a[212:215], a[180:183], v[212:215]\n"
"ds_read_b64_tr_b16 a[112:113], v37 offset:0\n"
"v_fma_f32 v52, -v52, v60, v60\n"
"v_fma_f32 v53, -v53, v61, v61\n"
"v_mfma_f32_16x16x32_bf16 v[216:219], a[216:219], a[180:183], v[216:219]\n"
"ds_read_b64_tr_b16 a[114:115], v39 offset:0\n"
"v_fma_f32 v54, -v54, v62, v62\n"
"v_fma_f32 v55, -v55, v63, v63\n"
"v_mfma_f32_16x16x32_bf16 v[220:223], a[220:223], a[180:183], v[220:223]\n"
"s_waitcnt lgkmcnt(7)\n"
"ds_read_b64_tr_b16 a[116:117], v38 offset:0\n"
"v_bfi_b32 v48, s70, v48, v128\n"
"v_bfi_b32 v49, s70, v49, v129\n"
"v_bfi_b32 v50, s70, v50, v130\n"
"v_mfma_f32_16x16x32_bf16 v[224:227], a[208:211], a[184:187], v[224:227]\n"
"ds_read_b64_tr_b16 a[118:119], v40 offset:0\n"
"v_bfi_b32 v51, s70, v51, v131\n"
"v_bfi_b32 v52, s70, v52, v132\n"
"v_mfma_f32_16x16x32_bf16 v[228:231], a[212:215], a[184:187], v[228:231]\n"
"ds_read_b64_tr_b16 a[120:121], v37 offset:512\n"
"v_bfi_b32 v53, s70, v53, v133\n"
"v_bfi_b32 v54, s70, v54, v134\n"
"v_bfi_b32 v55, s70, v55, v135\n"
"v_mfma_f32_16x16x32_bf16 v[232:235], a[216:219], a[184:187], v[232:235]\n"
"ds_read_b64_tr_b16 a[122:123], v39 offset:512\n"
"v_cvt_pk_bf16_f32 v116, v48, v49\n"
"v_cvt_pk_bf16_f32 v117, v50, v51\n"
"v_mfma_f32_16x16x32_bf16 v[236:239], a[220:223], a[184:187], v[236:239]\n"
"s_waitcnt lgkmcnt(9)\n"
"ds_read_b64_tr_b16 a[124:125], v38 offset:512\n"
"v_cvt_pk_bf16_f32 v118, v52, v53\n"
"v_cvt_pk_bf16_f32 v119, v54, v55\n"
"v_mfma_f32_16x16x32_bf16 v[240:243], a[208:211], a[188:191], v[240:243]\n"
"ds_read_b64_tr_b16 a[126:127], v40 offset:512\n"
"v_xor_b32 v124, 0, %[vOw]\n"
"v_xor_b32 v125, 32, %[vOw]\n"
"v_mfma_f32_16x16x32_bf16 v[244:247], a[212:215], a[188:191], v[244:247]\n"
"ds_write_b128 %[vZw], v[116:119] offset:12288\n"
"v_xor_b32 v126, 64, %[vOw]\n"
"v_xor_b32 v127, 96, %[vOw]\n"
"v_mfma_f32_16x16x32_bf16 v[248:251], a[216:219], a[188:191], v[248:251]\n"
"v_mul_f32 v144, s68, v144\n"
"v_mul_f32 v145, s68, v145\n"
"v_mfma_f32_16x16x32_bf16 v[252:255], a[220:223], a[188:191], v[252:255]\n"
"s_waitcnt lgkmcnt(9)\n"
"v_mul_f32 v146, s68, v146\n"
"v_mul_f32 v147, s68, v147\n"
"v_mul_f32 v148, s68, v148\n"
"v_mfma_f32_32x32x16_bf16 v[80:95], a[0:3], a[224:227], 0\n"
"v_mul_f32 v149, s68, v149\n"
"v_mul_f32 v150, s68, v150\n"
"v_mul_f32 v151, s68, v151\n"
"v_mul_f32 v152, s69, v152\n"
"v_mul_f32 v153, s69, v153\n"
"v_mfma_f32_32x32x16_bf16 v[96:111], a[4:7], a[224:227], 0\n"
"v_mul_f32 v154, s69, v154\n"
"v_mul_f32 v155, s69, v155\n"
"v_mul_f32 v156, s69, v156\n"
"v_mul_f32 v157, s69, v157\n"
"v_mfma_f32_32x32x16_bf16 v[80:95], a[8:11], a[228:231], v[80:95]\n"
"v_mul_f32 v158, s69, v158\n"
"v_mul_f32 v159, s69, v159\n"
"v_exp_f32 v64, -|v144|\n"
"v_exp_f32 v65, -|v145|\n"
"v_mfma_f32_32x32x16_bf16 v[96:111], a[12:15], a[228:231], v[96:111]\n"
"v_exp_f32 v66, -|v146|\n"
"v_exp_f32 v67, -|v147|\n"
"v_mfma_f32_32x32x16_bf16 v[80:95], a[16:19], a[232:235], v[80:95]\n"
"v_exp_f32 v68, -|v148|\n"
"v_exp_f32 v69, -|v149|\n"
"v_exp_f32 v70, -|v150|\n"
"v_mfma_f32_32x32x16_bf16 v[96:111], a[20:23], a[232:235], v[96:111]\n"
"v_exp_f32 v71, -|v151|\n"
"v_exp_f32 v152, -v152\n"
"v_mfma_f32_32x32x16_bf16 v[80:95], a[24:27], a[236:239], v[80:95]\n"
"v_exp_f32 v153, -v153\n"
"v_exp_f32 v154, -v154\n"
"v_mfma_f32_32x32x16_bf16 v[96:111], a[28:31], a[236:239], v[96:111]\n"
"v_exp_f32 v155, -v155\n"
"v_exp_f32 v156, -v156\n"
"v_exp_f32 v157, -v157\n"
"v_mfma_f32_32x32x16_bf16 v[80:95], a[32:35], a[240:243], v[80:95]\n"
"ds_read_b128 a[160:163], %[vZr] offset:6144\n"
"ds_read_b128 a[164:167], %[vZr] offset:6656\n"
"v_exp_f32 v158, -v158\n"
"v_exp_f32 v159, -v159\n"
"v_mfma_f32_32x32x16_bf16 v[96:111], a[36:39], a[240:243], v[96:111]\n"
"ds_read_b128 a[168:171], %[vZr] offset:26624\n"
"ds_read_b128 a[172:175], %[vZr] offset:27136\n"
"v_add_f32 v72, 1.0, v64\n"
"v_add_f32 v73, 1.0, v65\n"
"v_add_f32 v74, 1.0, v66\n"
"v_add_f32 v75, 1.0, v67\n"
"v_mfma_f32_32x32x16_bf16 v[80:95], a[40:43], a[244:247], v[80:95]\n"
"ds_read_b128 a[176:179], v112 offset:6144\n"
"ds_read_b128 a[180:183], v112 offset:6656\n"
"v_add_f32 v76, 1.0, v68\n"
"v_add_f32 v77, 1.0, v69\n"
"v_add_f32 v78, 1.0, v70\n"
"v_add_f32 v79, 1.0, v71\n"
"v_fmac_f32 v72, v72, v152\n"
"v_mfma_f32_32x32x16_bf16 v[96:111], a[44:47], a[244:247], v[96:111]\n"
"ds_read_b128 a[184:187], v112 offset:26624\n"
"ds_read_b128 a[188:191], v112 offset:27136\n"
"v_fmac_f32 v73, v73, v153\n"
"v_fmac_f32 v74, v74, v154\n"
"v_fmac_f32 v75, v75, v155\n"
"v_fmac_f32 v76, v76, v156\n"
"v_fmac_f32 v77, v77, v157\n"
"v_mfma_f32_32x32x16_bf16 v[80:95], a[48:51], a[248:251], v[80:95]\n"
"v_fmac_f32 v78, v78, v158\n"
"v_fmac_f32 v79, v79, v159\n"
"v_rcp_f32 v72, v72\n"
"v_rcp_f32 v73, v73\n"
"v_mfma_f32_32x32x16_bf16 v[96:111], a[52:55], a[248:251], v[96:111]\n"
"v_rcp_f32 v74, v74\n"
"v_rcp_f32 v75, v75\n"
"v_mfma_f32_32x32x16_bf16 v[80:95], a[56:59], a[252:255], v[80:95]\n"
"v_rcp_f32 v76, v76\n"
"v_rcp_f32 v77, v77\n"
"v_mfma_f32_32x32x16_bf16 v[96:111], a[60:63], a[252:255], v[96:111]\n"
"v_rcp_f32 v78, v78\n"
"v_rcp_f32 v79, v79\n"
"v_fma_f32 v64, -v64, v72, v72\n"
"v_fma_f32 v65, -v65, v73, v73\n"
"v_fma_f32 v66, -v66, v74, v74\n"
"v_fma_f32 v67, -v67, v75, v75\n"
"v_fma_f32 v68, -v68, v76, v76\n"
"v_fma_f32 v69, -v69, v77, v77\n"
"v_fma_f32 v70, -v70, v78, v78\n"
"v_fma_f32 v71, -v71, v79, v79\n"
"v_bfi_b32 v64, s70, v64, v144\n"
"v_bfi_b32 v65, s70, v65, v145\n"
"v_bfi_b32 v66, s70, v66, v146\n"
"v_bfi_b32 v67, s70, v67, v147\n"
"v_bfi_b32 v68, s70, v68, v148\n"
"v_bfi_b32 v69, s70, v69, v149\n"
"v_bfi_b32 v70, s70, v70, v150\n"
"v_bfi_b32 v71, s70, v71, v151\n"
"v_cvt_pk_bf16_f32 v120, v64, v65\n"
"v_cvt_pk_bf16_f32 v121, v66, v67\n"
"v_cvt_pk_bf16_f32 v122, v68, v69\n"
"v_cvt_pk_bf16_f32 v123, v70, v71\n"
"ds_write_b128 %[vZw], v[120:123] offset:13312\n"
"s_waitcnt lgkmcnt(1)\n"
"v_mfma_f32_32x32x16_bf16 v[16:31], a[0:3], a[160:163], 0\n"
"v_mul_f32 v160, s68, v160\n"
"v_mul_f32 v161, s68, v161\n"
"v_mul_f32 v162, s68, v162\n"
"v_mul_f32 v163, s68, v163\n"
"v_mul_f32 v164, s68, v164\n"
"v_mul_f32 v165, s68, v165\n"
"v_mul_f32 v166, s68, v166\n"
"v_mfma_f32_32x32x16_bf16 v[32:47], a[4:7], a[160:163], 0\n"
"v_mul_f32 v167, s68, v167\n"
"v_mul_f32 v168, s69, v168\n"
"v_mul_f32 v169, s69, v169\n"
"v_mul_f32 v170, s69, v170\n"
"v_mul_f32 v171, s69, v171\n"
"v_mul_f32 v172, s69, v172\n"
"v_mfma_f32_32x32x16_bf16 v[16:31], a[8:11], a[164:167], v[16:31]\n"
"v_mul_f32 v173, s69, v173\n"
"v_mul_f32 v174, s69, v174\n"
"v_mul_f32 v175, s69, v175\n"
"v_exp_f32 v48, -|v160|\n"
"v_exp_f32 v49, -|v161|\n"
"v_mfma_f32_32x32x16_bf16 v[32:47], a[12:15], a[164:167], v[32:47]\n"
"s_waitcnt lgkmcnt(0)\n"
"s_barrier\n"
"ds_read_b128 a[128:131], %[vZr] offset:12288\n"
"ds_read_b128 a[132:135], %[vZr] offset:12800\n"
"ds_read_b128 a[136:139], %[vZr] offset:32768\n"
"ds_read_b128 a[140:143], %[vZr] offset:33280\n"
"v_exp_f32 v50, -|v162|\n"
"v_exp_f32 v51, -|v163|\n"
"v_exp_f32 v52, -|v164|\n"
"v_mfma_f32_32x32x16_bf16 v[16:31], a[16:19], a[168:171], v[16:31]\n"
"ds_read_b128 a[144:147], v112 offset:12288\n"
"ds_read_b128 a[148:151], v112 offset:12800\n"
"ds_read_b128 a[152:155], v112 offset:32768\n"
"ds_read_b128 a[156:159], v112 offset:33280\n"
"v_exp_f32 v53, -|v165|\n"
"v_exp_f32 v54, -|v166|\n"
"v_exp_f32 v55, -|v167|\n"
"v_mfma_f32_32x32x16_bf16 v[32:47], a[20:23], a[168:171], v[32:47]\n"
"v_exp_f32 v168, -v168\n"
"v_exp_f32 v169, -v169\n"
"v_exp_f32 v170, -v170\n"
"s_waitcnt lgkmcnt(0)\n"
"v_mfma_f32_32x32x16_bf16 v[80:95], a[64:67], a[128:131], v[80:95]\n"
"v_exp_f32 v171, -v171\n"
"v_exp_f32 v172, -v172\n"
"v_exp_f32 v173, -v173\n"
"v_mfma_f32_32x32x16_bf16 v[96:111], a[68:71], a[128:131], v[96:111]\n"
"v_exp_f32 v174, -v174\n"
"v_exp_f32 v175, -v175\n"
"v_add_f32 v56, 1.0, v48\n"
"v_add_f32 v57, 1.0, v49\n"
"v_mfma_f32_32x32x16_bf16 v[80:95], a[72:75], a[132:135], v[80:95]\n"
"ds_read_b128 a[224:227], %[vZr] offset:8192\n"
"ds_read_b128 a[228:231], %[vZr] offset:8704\n"
"v_add_f32 v58, 1.0, v50\n"
"v_add_f32 v59, 1.0, v51\n"
"v_add_f32 v60, 1.0, v52\n"
"v_add_f32 v61, 1.0, v53\n"
"v_add_f32 v62, 1.0, v54\n"
"v_add_f32 v63, 1.0, v55\n"
"v_fmac_f32 v56, v56, v168\n"
"v_mfma_f32_32x32x16_bf16 v[96:111], a[76:79], a[132:135], v[96:111]\n"
"ds_read_b128 a[232:235], %[vZr] offset:28672\n"
"ds_read_b128 a[236:239], %[vZr] offset:29184\n"
"v_fmac_f32 v57, v57, v169\n"
"v_fmac_f32 v58, v58, v170\n"
"v_fmac_f32 v59, v59, v171\n"
"v_fmac_f32 v60, v60, v172\n"
"v_fmac_f32 v61, v61, v173\n"
"v_fmac_f32 v62, v62, v174\n"
"v_mfma_f32_32x32x16_bf16 v[80:95], a[80:83], a[136:139], v[80:95]\n"
"ds_read_b128 a[240:243], v112 offset:8192\n"
"ds_read_b128 a[244:247], v112 offset:8704\n"
"v_fmac_f32 v63, v63, v175\n"
"v_rcp_f32 v56, v56\n"
"v_rcp_f32 v57, v57\n"
"v_rcp_f32 v58, v58\n"
"v_mfma_f32_32x32x16_bf16 v[96:111], a[84:87], a[136:139], v[96:111]\n"
"ds_read_b128 a[248:251], v112 offset:28672\n"
"ds_read_b128 a[252:255], v112 offset:29184\n"
"v_rcp_f32 v59, v59\n"
"v_rcp_f32 v60, v60\n"
"v_rcp_f32 v61, v61\n"
"v_mfma_f32_32x32x16_bf16 v[80:95], a[88:91], a[140:143], v[80:95]\n"
"v_rcp_f32 v62, v62\n"
"v_rcp_f32 v63, v63\n"
"v_fma_f32 v48, -v48, v56, v56\n"
"v_fma_f32 v49, -v49, v57, v57\n"
"v_mfma_f32_32x32x16_bf16 v[96:111], a[92:95], a[140:143], v[96:111]\n"
"v_fma_f32 v50, -v50, v58, v58\n"
"v_fma_f32 v51, -v51, v59, v59\n"
"v_fma_f32 v52, -v52, v60, v60\n"
"v_fma_f32 v53, -v53, v61, v61\n"
"v_fma_f32 v54, -v54, v62, v62\n"
"v_fma_f32 v55, -v55, v63, v63\n"
"v_mfma_f32_32x32x16_bf16 v[80:95], a[96:99], a[144:147], v[80:95]\n"
"v_bfi_b32 v48, s70, v48, v160\n"
"v_bfi_b32 v49, s70, v49, v161\n"
"v_bfi_b32 v50, s70, v50, v162\n"
"v_bfi_b32 v51, s70, v51, v163\n"
"v_bfi_b32 v52, s70, v52, v164\n"
"v_bfi_b32 v53, s70, v53, v165\n"
"v_mfma_f32_32x32x16_bf16 v[96:111], a[100:103], a[144:147], v[96:111]\n"
"v_bfi_b32 v54, s70, v54, v166\n"
"v_bfi_b32 v55, s70, v55, v167\n"
"v_cvt_pk_bf16_f32 v116, v48, v49\n"
"v_cvt_pk_bf16_f32 v117, v50, v51\n"
"v_cvt_pk_bf16_f32 v118, v52, v53\n"
"v_cvt_pk_bf16_f32 v119, v54, v55\n"
"v_mfma_f32_32x32x16_bf16 v[80:95], a[104:107], a[148:151], v[80:95]\n"
"v_mul_f32 v176, s68, v176\n"
"v_mul_f32 v177, s68, v177\n"
"v_mul_f32 v178, s68, v178\n"
"v_mul_f32 v179, s68, v179\n"
"v_mul_f32 v180, s68, v180\n"
"v_mul_f32 v181, s68, v181\n"
"v_mfma_f32_32x32x16_bf16 v[96:111], a[108:111], a[148:151], v[96:111]\n"
"ds_write_b128 %[vZw], v[116:119] offset:14336\n"
"v_mul_f32 v182, s68, v182\n"
"v_mul_f32 v183, s68, v183\n"
"v_mul_f32 v184, s69, v184\n"
"v_mul_f32 v185, s69, v185\n"
"v_mul_f32 v186, s69, v186\n"
"v_mul_f32 v187, s69, v187\n"
"v_mfma_f32_32x32x16_bf16 v[80:95], a[112:115], a[152:155], v[80:95]\n"
"v_mul_f32 v188, s69, v188\n"
"v_mul_f32 v189, s69, v189\n"
"v_mul_f32 v190, s69, v190\n"
"v_mul_f32 v191, s69, v191\n"
"v_exp_f32 v64, -|v176|\n"
"v_mfma_f32_32x32x16_bf16 v[96:111], a[116:119], a[152:155], v[96:111]\n"
"v_exp_f32 v65, -|v177|\n"
"v_exp_f32 v66, -|v178|\n"
"v_exp_f32 v67, -|v179|\n"
"v_mfma_f32_32x32x16_bf16 v[80:95], a[120:123], a[156:159], v[80:95]\n"
"v_exp_f32 v68, -|v180|\n"
"v_exp_f32 v69, -|v181|\n"
"v_exp_f32 v70, -|v182|\n"
"v_exp_f32 v71, -|v183|\n"
"v_mfma_f32_32x32x16_bf16 v[96:111], a[124:127], a[156:159], v[96:111]\n"
"v_exp_f32 v184, -v184\n"
"v_exp_f32 v185, -v185\n"
"v_exp_f32 v186, -v186\n"
"v_mfma_f32_32x32x16_bf16 v[16:31], a[24:27], a[172:175], v[16:31]\n"
"v_exp_f32 v187, -v187\n"
"v_exp_f32 v188, -v188\n"
"v_exp_f32 v189, -v189\n"
"v_mfma_f32_32x32x16_bf16 v[32:47], a[28:31], a[172:175], v[32:47]\n"
"v_exp_f32 v190, -v190\n"
"v_exp_f32 v191, -v191\n"
"v_add_f32 v72, 1.0, v64\n"
"v_mfma_f32_32x32x16_bf16 v[16:31], a[32:35], a[176:179], v[16:31]\n"
"v_add_f32 v73, 1.0, v65\n"
"v_add_f32 v74, 1.0, v66\n"
"v_add_f32 v75, 1.0, v67\n"
"v_add_f32 v76, 1.0, v68\n"
"v_add_f32 v77, 1.0, v69\n"
"v_add_f32 v78, 1.0, v70\n"
"v_add_f32 v79, 1.0, v71\n"
"v_mfma_f32_32x32x16_bf16 v[32:47], a[36:39], a[176:179], v[32:47]\n"
"v_fmac_f32 v72, v72, v184\n"
"v_cvt_pk_bf16_f32 v80, v80, v81\n"
"v_cvt_pk_bf16_f32 v81, v82, v83\n"
"v_cvt_pk_bf16_f32 v82, v84, v85\n"
"v_cvt_pk_bf16_f32 v83, v86, v87\n"
"v_mfma_f32_32x32x16_bf16 v[16:31], a[40:43], a[180:183], v[16:31]\n"
"ds_write_b128 v124, v[80:83]\n"
"v_fmac_f32 v73, v73, v185\n"
"v_fmac_f32 v74, v74, v186\n"
"v_fmac_f32 v75, v75, v187\n"
"v_fmac_f32 v76, v76, v188\n"
"v_fmac_f32 v77, v77, v189\n"
"v_fmac_f32 v78, v78, v190\n"
"v_mfma_f32_32x32x16_bf16 v[32:47], a[44:47], a[180:183], v[32:47]\n"
"v_fmac_f32 v79, v79, v191\n"
"v_rcp_f32 v72, v72\n"
"v_cvt_pk_bf16_f32 v88, v88, v89\n"
"v_cvt_pk_bf16_f32 v89, v90, v91\n"
"v_cvt_pk_bf16_f32 v90, v92, v93\n"
"v_mfma_f32_32x32x16_bf16 v[16:31], a[48:51], a[184:187], v[16:31]\n"
"v_cvt_pk_bf16_f32 v91, v94, v95\n"
"v_rcp_f32 v73, v73\n"
"v_rcp_f32 v74, v74\n"
"v_rcp_f32 v75, v75\n"
"v_mfma_f32_32x32x16_bf16 v[32:47], a[52:55], a[184:187], v[32:47]\n"
"ds_write_b128 v125, v[88:91]\n"
"v_rcp_f32 v76, v76\n"
"v_rcp_f32 v77, v77\n"
"v_rcp_f32 v78, v78\n"
"v_mfma_f32_32x32x16_bf16 v[16:31], a[56:59], a[188:191], v[16:31]\n"
"v_rcp_f32 v79, v79\n"
"v_fma_f32 v64, -v64, v72, v72\n"
"v_cvt_pk_bf16_f32 v96, v96, v97\n"
"v_cvt_pk_bf16_f32 v97, v98, v99\n"
"v_mfma_f32_32x32x16_bf16 v[32:47], a[60:63], a[188:191], v[32:47]\n"
"v_cvt_pk_bf16_f32 v98, v100, v101\n"
"v_cvt_pk_bf16_f32 v99, v102, v103\n"
"ds_write_b128 v126, v[96:99]\n"
"v_fma_f32 v65, -v65, v73, v73\n"
"v_fma_f32 v66, -v66, v74, v74\n"
"v_fma_f32 v67, -v67, v75, v75\n"
"v_fma_f32 v68, -v68, v76, v76\n"
"v_fma_f32 v69, -v69, v77, v77\n"
"v_fma_f32 v70, -v70, v78, v78\n"
"v_fma_f32 v71, -v71, v79, v79\n"
"v_bfi_b32 v64, s70, v64, v176\n"
"v_cvt_pk_bf16_f32 v104, v104, v105\n"
"v_cvt_pk_bf16_f32 v105, v106, v107\n"
"v_cvt_pk_bf16_f32 v106, v108, v109\n"
"v_cvt_pk_bf16_f32 v107, v110, v111\n"
"ds_write_b128 v127, v[104:107]\n"
"v_bfi_b32 v65, s70, v65, v177\n"
"v_bfi_b32 v66, s70, v66, v178\n"
"v_bfi_b32 v67, s70, v67, v179\n"
"v_bfi_b32 v68, s70, v68, v180\n"
"v_bfi_b32 v69, s70, v69, v181\n"
"v_bfi_b32 v70, s70, v70, v182\n"
"v_bfi_b32 v71, s70, v71, v183\n"
"v_cvt_pk_bf16_f32 v120, v64, v65\n"
"v_cvt_pk_bf16_f32 v121, v66, v67\n"
"v_cvt_pk_bf16_f32 v122, v68, v69\n"
"v_cvt_pk_bf16_f32 v123, v70, v71\n"
"ds_write_b128 %[vZw], v[120:123] offset:15360\n"
"s_waitcnt lgkmcnt(6)\n"
"v_mfma_f32_32x32x16_bf16 v[128:143], a[0:3], a[224:227], 0\n"
"v_mul_f32 v192, s68, v192\n"
"v_mul_f32 v193, s68, v193\n"
"v_mul_f32 v194, s68, v194\n"
"v_mfma_f32_32x32x16_bf16 v[144:159], a[4:7], a[224:227], 0\n"
"ds_read_b128 v[84:87], %[vZw] offset:0\n"
"ds_read_b128 v[92:95], %[vZw] offset:1024\n"
"ds_read_b128 v[100:103], %[vZw] offset:2048\n"
"ds_read_b128 v[108:111], %[vZw] offset:3072\n"
"v_mul_f32 v195, s68, v195\n"
"v_mul_f32 v196, s68, v196\n"
"v_mul_f32 v197, s68, v197\n"
"v_mul_f32 v198, s68, v198\n"
"v_mul_f32 v199, s68, v199\n"
"v_mul_f32 v200, s69, v200\n"
"v_mul_f32 v201, s69, v201\n"
"v_mfma_f32_32x32x16_bf16 v[128:143], a[8:11], a[228:231], v[128:143]\n"
"v_mul_f32 v202, s69, v202\n"
"v_mul_f32 v203, s69, v203\n"
"v_mul_f32 v204, s69, v204\n"
"v_mul_f32 v205, s69, v205\n"
"v_mul_f32 v206, s69, v206\n"
"v_mul_f32 v207, s69, v207\n"
"v_exp_f32 v48, -|v192|\n"
"v_mfma_f32_32x32x16_bf16 v[144:159], a[12:15], a[228:231], v[144:159]\n"
"s_waitcnt lgkmcnt(4)\n"
"s_barrier\n"
"ds_read_b128 a[128:131], %[vZr] offset:14336\n"
"ds_read_b128 a[132:135], %[vZr] offset:14848\n"
"ds_read_b128 a[136:139], %[vZr] offset:34816\n"
"ds_read_b128 a[140:143], %[vZr] offset:35328\n"
"v_exp_f32 v49, -|v193|\n"
"v_exp_f32 v50, -|v194|\n"
"v_exp_f32 v51, -|v195|\n"
"v_mfma_f32_32x32x16_bf16 v[128:143], a[16:19], a[232:235], v[128:143]\n"
"ds_read_b128 a[144:147], v112 offset:14336\n"
"ds_read_b128 a[148:151], v112 offset:14848\n"
"ds_read_b128 a[152:155], v112 offset:34816\n"
"ds_read_b128 a[156:159], v112 offset:35328\n"
"v_exp_f32 v52, -|v196|\n"
"v_exp_f32 v53, -|v197|\n"
"v_exp_f32 v54, -|v198|\n"
"v_mfma_f32_32x32x16_bf16 v[144:159], a[20:23], a[232:235], v[144:159]\n"
"v_exp_f32 v55, -|v199|\n"
"v_exp_f32 v200, -v200\n"
"v_exp_f32 v201, -v201\n"
"v_exp_f32 v202, -v202\n"
"s_waitcnt lgkmcnt(0)\n"
"v_mfma_f32_32x32x16_bf16 v[16:31], a[64:67], a[128:131], v[16:31]\n"
"v_exp_f32 v203, -v203\n"
"v_exp_f32 v204, -v204\n"
"v_exp_f32 v205, -v205\n"
"v_mfma_f32_32x32x16_bf16 v[32:47], a[68:71], a[128:131], v[32:47]\n"
"v_exp_f32 v206, -v206\n"
"v_exp_f32 v207, -v207\n"
"v_mfma_f32_32x32x16_bf16 v[16:31], a[72:75], a[132:135], v[16:31]\n"
"ds_read_b128 a[160:163], %[vZr] offset:10240\n"
"ds_read_b128 a[164:167], %[vZr] offset:10752\n"
"s_waitcnt lgkmcnt(13)\n"
"global_store_dwordx4 %[vO], v[84:87], s[96:97] offset:-4096 sc0 sc1\n"
"v_add_f32 v56, 1.0, v48\n"
"v_add_f32 v57, 1.0, v49\n"
"v_add_f32 v58, 1.0, v50\n"
"v_add_f32 v59, 1.0, v51\n"
"v_add_f32 v60, 1.0, v52\n"
"v_add_f32 v61, 1.0, v53\n"
"v_add_f32 v62, 1.0, v54\n"
"v_mfma_f32_32x32x16_bf16 v[32:47], a[76:79], a[132:135], v[32:47]\n"
"ds_read_b128 a[168:171], %[vZr] offset:30720\n"
"ds_read_b128 a[172:175], %[vZr] offset:31232\n"
"v_add_f32 v63, 1.0, v55\n"
"v_fmac_f32 v56, v56, v200\n"
"v_fmac_f32 v57, v57, v201\n"
"v_fmac_f32 v58, v58, v202\n"
"v_fmac_f32 v59, v59, v203\n"
"v_fmac_f32 v60, v60, v204\n"
"v_mfma_f32_32x32x16_bf16 v[16:31], a[80:83], a[136:139], v[16:31]\n"
"ds_read_b128 a[176:179], v112 offset:10240\n"
"ds_read_b128 a[180:183], v112 offset:10752\n"
"v_fmac_f32 v61, v61, v205\n"
"v_fmac_f32 v62, v62, v206\n"
"v_fmac_f32 v63, v63, v207\n"
"v_rcp_f32 v56, v56\n"
"v_rcp_f32 v57, v57\n"
"v_mfma_f32_32x32x16_bf16 v[32:47], a[84:87], a[136:139], v[32:47]\n"
"ds_read_b128 a[184:187], v112 offset:30720\n"
"ds_read_b128 a[188:191], v112 offset:31232\n"
"v_rcp_f32 v58, v58\n"
"v_rcp_f32 v59, v59\n"
"v_mfma_f32_32x32x16_bf16 v[16:31], a[88:91], a[140:143], v[16:31]\n"
"s_waitcnt lgkmcnt(15)\n"
"global_store_dwordx4 %[vO], v[92:95], s[96:97] offset:0 sc0 sc1\n"
"s_add_u32 s96, s96, 8192\n"
"s_addc_u32 s97, s97, 0\n"
"v_rcp_f32 v60, v60\n"
"v_rcp_f32 v61, v61\n"
"v_rcp_f32 v62, v62\n"
"v_mfma_f32_32x32x16_bf16 v[32:47], a[92:95], a[140:143], v[32:47]\n"
"v_rcp_f32 v63, v63\n"
"v_fma_f32 v48, -v48, v56, v56\n"
"v_fma_f32 v49, -v49, v57, v57\n"
"v_fma_f32 v50, -v50, v58, v58\n"
"v_fma_f32 v51, -v51, v59, v59\n"
"v_mfma_f32_32x32x16_bf16 v[16:31], a[96:99], a[144:147], v[16:31]\n"
"v_fma_f32 v52, -v52, v60, v60\n"
"v_fma_f32 v53, -v53, v61, v61\n"
"v_fma_f32 v54, -v54, v62, v62\n"
"v_fma_f32 v55, -v55, v63, v63\n"
"v_bfi_b32 v48, s70, v48, v192\n"
"v_bfi_b32 v49, s70, v49, v193\n"
"v_bfi_b32 v50, s70, v50, v194\n"
"v_mfma_f32_32x32x16_bf16 v[32:47], a[100:103], a[144:147], v[32:47]\n"
"v_bfi_b32 v51, s70, v51, v195\n"
"v_bfi_b32 v52, s70, v52, v196\n"
"v_bfi_b32 v53, s70, v53, v197\n"
"v_bfi_b32 v54, s70, v54, v198\n"
"v_bfi_b32 v55, s70, v55, v199\n"
"v_mfma_f32_32x32x16_bf16 v[16:31], a[104:107], a[148:151], v[16:31]\n"
"s_waitcnt lgkmcnt(15)\n"
"global_store_dwordx4 %[vO], v[100:103], s[96:97] offset:-4096 sc0 sc1\n"
"v_cvt_pk_bf16_f32 v116, v48, v49\n"
"v_cvt_pk_bf16_f32 v117, v50, v51\n"
"v_cvt_pk_bf16_f32 v118, v52, v53\n"
"v_cvt_pk_bf16_f32 v119, v54, v55\n"
"v_mul_f32 v208, s68, v208\n"
"v_mfma_f32_32x32x16_bf16 v[32:47], a[108:111], a[148:151], v[32:47]\n"
"ds_write_b128 %[vZw], v[116:119] offset:16384\n"
"v_mul_f32 v209, s68, v209\n"
"v_mul_f32 v210, s68, v210\n"
"v_mul_f32 v211, s68, v211\n"
"v_mul_f32 v212, s68, v212\n"
"v_mul_f32 v213, s68, v213\n"
"v_mul_f32 v214, s68, v214\n"
"v_mul_f32 v215, s68, v215\n"
"v_mfma_f32_32x32x16_bf16 v[16:31], a[112:115], a[152:155], v[16:31]\n"
"v_mul_f32 v216, s69, v216\n"
"v_mul_f32 v217, s69, v217\n"
"v_mul_f32 v218, s69, v218\n"
"v_mul_f32 v219, s69, v219\n"
"v_mul_f32 v220, s69, v220\n"
"v_mfma_f32_32x32x16_bf16 v[32:47], a[116:119], a[152:155], v[32:47]\n"
"s_waitcnt lgkmcnt(15)\n"
"global_store_dwordx4 %[vO], v[108:111], s[96:97] offset:0 sc0 sc1\n"
"v_mul_f32 v221, s69, v221\n"
"v_mul_f32 v222, s69, v222\n"
"v_mul_f32 v223, s69, v223\n"
"v_exp_f32 v64, -|v208|\n"
"v_mfma_f32_32x32x16_bf16 v[16:31], a[120:123], a[156:159], v[16:31]\n"
"s_add_u32 s96, s96, 8192\n"
"s_addc_u32 s97, s97, 0\n"
"v_exp_f32 v65, -|v209|\n"
"v_exp_f32 v66, -|v210|\n"
"v_exp_f32 v67, -|v211|\n"
"v_mfma_f32_32x32x16_bf16 v[32:47], a[124:127], a[156:159], v[32:47]\n"
"v_exp_f32 v68, -|v212|\n"
"v_exp_f32 v69, -|v213|\n"
"v_exp_f32 v70, -|v214|\n"
"v_exp_f32 v71, -|v215|\n"
"v_mfma_f32_32x32x16_bf16 v[128:143], a[24:27], a[236:239], v[128:143]\n"
"v_exp_f32 v216, -v216\n"
"v_exp_f32 v217, -v217\n"
"v_exp_f32 v218, -v218\n"
"v_mfma_f32_32x32x16_bf16 v[144:159], a[28:31], a[236:239], v[144:159]\n"
"v_exp_f32 v219, -v219\n"
"v_exp_f32 v220, -v220\n"
"v_exp_f32 v221, -v221\n"
"v_mfma_f32_32x32x16_bf16 v[128:143], a[32:35], a[240:243], v[128:143]\n"
"v_exp_f32 v222, -v222\n"
"v_exp_f32 v223, -v223\n"
"v_add_f32 v72, 1.0, v64\n"
"v_add_f32 v73, 1.0, v65\n"
"v_add_f32 v74, 1.0, v66\n"
"v_mfma_f32_32x32x16_bf16 v[144:159], a[36:39], a[240:243], v[144:159]\n"
"v_add_f32 v75, 1.0, v67\n"
"v_add_f32 v76, 1.0, v68\n"
"v_add_f32 v77, 1.0, v69\n"
"v_add_f32 v78, 1.0, v70\n"
"v_add_f32 v79, 1.0, v71\n"
"v_fmac_f32 v72, v72, v216\n"
"v_cvt_pk_bf16_f32 v16, v16, v17\n"
"v_mfma_f32_32x32x16_bf16 v[128:143], a[40:43], a[244:247], v[128:143]\n"
"v_cvt_pk_bf16_f32 v17, v18, v19\n"
"v_cvt_pk_bf16_f32 v18, v20, v21\n"
"v_cvt_pk_bf16_f32 v19, v22, v23\n"
"v_fmac_f32 v73, v73, v217\n"
"v_fmac_f32 v74, v74, v218\n"
"v_mfma_f32_32x32x16_bf16 v[144:159], a[44:47], a[244:247], v[144:159]\n"
"ds_write_b128 v124, v[16:19]\n"
"v_fmac_f32 v75, v75, v219\n"
"v_fmac_f32 v76, v76, v220\n"
"v_fmac_f32 v77, v77, v221\n"
"v_fmac_f32 v78, v78, v222\n"
"v_fmac_f32 v79, v79, v223\n"
"v_rcp_f32 v72, v72\n"
"v_mfma_f32_32x32x16_bf16 v[128:143], a[48:51], a[248:251], v[128:143]\n"
"v_cvt_pk_bf16_f32 v24, v24, v25\n"
"v_cvt_pk_bf16_f32 v25, v26, v27\n"
"v_cvt_pk_bf16_f32 v26, v28, v29\n"
"v_cvt_pk_bf16_f32 v27, v30, v31\n"
"v_rcp_f32 v73, v73\n"
"v_mfma_f32_32x32x16_bf16 v[144:159], a[52:55], a[248:251], v[144:159]\n"
"ds_write_b128 v125, v[24:27]\n"
"v_rcp_f32 v74, v74\n"
"v_rcp_f32 v75, v75\n"
"v_rcp_f32 v76, v76\n"
"v_mfma_f32_32x32x16_bf16 v[128:143], a[56:59], a[252:255], v[128:143]\n"
"v_rcp_f32 v77, v77\n"
"v_rcp_f32 v78, v78\n"
"v_rcp_f32 v79, v79\n"
"v_fma_f32 v64, -v64, v72, v72\n"
"v_mfma_f32_32x32x16_bf16 v[144:159], a[60:63], a[252:255], v[144:159]\n"
"v_cvt_pk_bf16_f32 v32, v32, v33\n"
"v_cvt_pk_bf16_f32 v33, v34, v35\n"
"v_cvt_pk_bf16_f32 v34, v36, v37\n"
"v_cvt_pk_bf16_f32 v35, v38, v39\n"
"ds_write_b128 v126, v[32:35]\n"
"v_fma_f32 v65, -v65, v73, v73\n"
"v_fma_f32 v66, -v66, v74, v74\n"
"v_fma_f32 v67, -v67, v75, v75\n"
"v_fma_f32 v68, -v68, v76, v76\n"
"v_fma_f32 v69, -v69, v77, v77\n"
"v_fma_f32 v70, -v70, v78, v78\n"
"v_fma_f32 v71, -v71, v79, v79\n"
"v_bfi_b32 v64, s70, v64, v208\n"
"v_cvt_pk_bf16_f32 v40, v40, v41\n"
"v_cvt_pk_bf16_f32 v41, v42, v43\n"
"v_cvt_pk_bf16_f32 v42, v44, v45\n"
"v_cvt_pk_bf16_f32 v43, v46, v47\n"
"ds_write_b128 v127, v[40:43]\n"
"v_bfi_b32 v65, s70, v65, v209\n"
"v_bfi_b32 v66, s70, v66, v210\n"
"v_bfi_b32 v67, s70, v67, v211\n"
"v_bfi_b32 v68, s70, v68, v212\n"
"v_bfi_b32 v69, s70, v69, v213\n"
"v_bfi_b32 v70, s70, v70, v214\n"
"v_bfi_b32 v71, s70, v71, v215\n"
"v_cvt_pk_bf16_f32 v120, v64, v65\n"
"v_cvt_pk_bf16_f32 v121, v66, v67\n"
"v_cvt_pk_bf16_f32 v122, v68, v69\n"
"v_cvt_pk_bf16_f32 v123, v70, v71\n"
"ds_write_b128 %[vZw], v[120:123] offset:17408\n"
"s_waitcnt lgkmcnt(6)\n"
"v_mfma_f32_32x32x16_bf16 v[160:175], a[0:3], a[160:163], 0\n"
"v_mul_f32 v224, s68, v224\n"
"v_mul_f32 v225, s68, v225\n"
"v_mul_f32 v226, s68, v226\n"
"v_mfma_f32_32x32x16_bf16 v[176:191], a[4:7], a[160:163], 0\n"
"ds_read_b128 v[20:23], %[vZw] offset:0\n"
"ds_read_b128 v[28:31], %[vZw] offset:1024\n"
"ds_read_b128 v[36:39], %[vZw] offset:2048\n"
"ds_read_b128 v[44:47], %[vZw] offset:3072\n"
"v_mul_f32 v227, s68, v227\n"
"v_mul_f32 v228, s68, v228\n"
"v_mul_f32 v229, s68, v229\n"
"v_mul_f32 v230, s68, v230\n"
"v_mul_f32 v231, s68, v231\n"
"v_mul_f32 v232, s69, v232\n"
"v_mul_f32 v233, s69, v233\n"
"v_mfma_f32_32x32x16_bf16 v[160:175], a[8:11], a[164:167], v[160:175]\n"
"v_mul_f32 v234, s69, v234\n"
"v_mul_f32 v235, s69, v235\n"
"v_mul_f32 v236, s69, v236\n"
"v_mul_f32 v237, s69, v237\n"
"v_mul_f32 v238, s69, v238\n"
"v_mul_f32 v239, s69, v239\n"
"v_exp_f32 v48, -|v224|\n"
"v_mfma_f32_32x32x16_bf16 v[176:191], a[12:15], a[164:167], v[176:191]\n"
"s_waitcnt lgkmcnt(4)\n"
"s_barrier\n"
"ds_read_b128 a[128:131], %[vZr] offset:16384\n"
"ds_read_b128 a[132:135], %[vZr] offset:16896\n"
"ds_read_b128 a[136:139], %[vZr] offset:36864\n"
"ds_read_b128 a[140:143], %[vZr] offset:37376\n"
"v_exp_f32 v49, -|v225|\n"
"v_exp_f32 v50, -|v226|\n"
"v_exp_f32 v51, -|v227|\n"
"v_mfma_f32_32x32x16_bf16 v[160:175], a[16:19], a[168:171], v[160:175]\n"
"ds_read_b128 a[144:147], v112 offset:16384\n"
"ds_read_b128 a[148:151], v112 offset:16896\n"
"ds_read_b128 a[152:155], v112 offset:36864\n"
"ds_read_b128 a[156:159], v112 offset:37376\n"
"v_exp_f32 v52, -|v228|\n"
"v_exp_f32 v53, -|v229|\n"
"v_exp_f32 v54, -|v230|\n"
"v_mfma_f32_32x32x16_bf16 v[176:191], a[20:23], a[168:171], v[176:191]\n"
"v_exp_f32 v55, -|v231|\n"
"v_exp_f32 v232, -v232\n"
"v_exp_f32 v233, -v233\n"
"v_exp_f32 v234, -v234\n"
"s_waitcnt lgkmcnt(0)\n"
"v_mfma_f32_32x32x16_bf16 v[128:143], a[64:67], a[128:131], v[128:143]\n"
"v_exp_f32 v235, -v235\n"
"v_exp_f32 v236, -v236\n"
"v_exp_f32 v237, -v237\n"
"v_mfma_f32_32x32x16_bf16 v[144:159], a[68:71], a[128:131], v[144:159]\n"
"v_exp_f32 v238, -v238\n"
"v_exp_f32 v239, -v239\n"
"v_mfma_f32_32x32x16_bf16 v[128:143], a[72:75], a[132:135], v[128:143]\n"
"s_waitcnt lgkmcnt(11)\n"
"global_store_dwordx4 %[vO], v[20:23], s[96:97] offset:-4096 sc0 sc1\n"
"v_add_f32 v56, 1.0, v48\n"
"v_add_f32 v57, 1.0, v49\n"
"v_add_f32 v58, 1.0, v50\n"
"v_add_f32 v59, 1.0, v51\n"
"v_add_f32 v60, 1.0, v52\n"
"v_add_f32 v61, 1.0, v53\n"
"v_add_f32 v62, 1.0, v54\n"
"v_mfma_f32_32x32x16_bf16 v[144:159], a[76:79], a[132:135], v[144:159]\n"
"v_add_f32 v63, 1.0, v55\n"
"v_fmac_f32 v56, v56, v232\n"
"v_fmac_f32 v57, v57, v233\n"
"v_fmac_f32 v58, v58, v234\n"
"v_fmac_f32 v59, v59, v235\n"
"v_fmac_f32 v60, v60, v236\n"
"v_mfma_f32_32x32x16_bf16 v[128:143], a[80:83], a[136:139], v[128:143]\n"
"v_fmac_f32 v61, v61, v237\n"
"v_fmac_f32 v62, v62, v238\n"
"v_fmac_f32 v63, v63, v239\n"
"v_rcp_f32 v56, v56\n"
"v_rcp_f32 v57, v57\n"
"v_mfma_f32_32x32x16_bf16 v[144:159], a[84:87], a[136:139], v[144:159]\n"
"v_rcp_f32 v58, v58\n"
"v_rcp_f32 v59, v59\n"
"v_mfma_f32_32x32x16_bf16 v[128:143], a[88:91], a[140:143], v[128:143]\n"
"s_waitcnt lgkmcnt(10)\n"
"global_store_dwordx4 %[vO], v[28:31], s[96:97] offset:0 sc0 sc1\n"
"s_add_u32 s96, s96, 8192\n"
"s_addc_u32 s97, s97, 0\n"
"v_rcp_f32 v60, v60\n"
"v_rcp_f32 v61, v61\n"
"v_rcp_f32 v62, v62\n"
"v_mfma_f32_32x32x16_bf16 v[144:159], a[92:95], a[140:143], v[144:159]\n"
"v_rcp_f32 v63, v63\n"
"v_fma_f32 v48, -v48, v56, v56\n"
"v_fma_f32 v49, -v49, v57, v57\n"
"v_fma_f32 v50, -v50, v58, v58\n"
"v_fma_f32 v51, -v51, v59, v59\n"
"v_mfma_f32_32x32x16_bf16 v[128:143], a[96:99], a[144:147], v[128:143]\n"
"v_fma_f32 v52, -v52, v60, v60\n"
"v_fma_f32 v53, -v53, v61, v61\n"
"v_fma_f32 v54, -v54, v62, v62\n"
"v_fma_f32 v55, -v55, v63, v63\n"
"v_bfi_b32 v48, s70, v48, v224\n"
"v_bfi_b32 v49, s70, v49, v225\n"
"v_bfi_b32 v50, s70, v50, v226\n"
"v_mfma_f32_32x32x16_bf16 v[144:159], a[100:103], a[144:147], v[144:159]\n"
"v_bfi_b32 v51, s70, v51, v227\n"
"v_bfi_b32 v52, s70, v52, v228\n"
"v_bfi_b32 v53, s70, v53, v229\n"
"v_bfi_b32 v54, s70, v54, v230\n"
"v_bfi_b32 v55, s70, v55, v231\n"
"v_mfma_f32_32x32x16_bf16 v[128:143], a[104:107], a[148:151], v[128:143]\n"
"s_waitcnt lgkmcnt(9)\n"
"global_store_dwordx4 %[vO], v[36:39], s[96:97] offset:-4096 sc0 sc1\n"
"v_cvt_pk_bf16_f32 v116, v48, v49\n"
"v_cvt_pk_bf16_f32 v117, v50, v51\n"
"v_cvt_pk_bf16_f32 v118, v52, v53\n"
"v_cvt_pk_bf16_f32 v119, v54, v55\n"
"v_mul_f32 v240, s68, v240\n"
"v_mfma_f32_32x32x16_bf16 v[144:159], a[108:111], a[148:151], v[144:159]\n"
"ds_write_b128 %[vZw], v[116:119] offset:18432\n"
"v_mul_f32 v241, s68, v241\n"
"v_mul_f32 v242, s68, v242\n"
"v_mul_f32 v243, s68, v243\n"
"v_mul_f32 v244, s68, v244\n"
"v_mul_f32 v245, s68, v245\n"
"v_mul_f32 v246, s68, v246\n"
"v_mul_f32 v247, s68, v247\n"
"v_mfma_f32_32x32x16_bf16 v[128:143], a[112:115], a[152:155], v[128:143]\n"
"v_mul_f32 v248, s69, v248\n"
"v_mul_f32 v249, s69, v249\n"
"v_mul_f32 v250, s69, v250\n"
"v_mul_f32 v251, s69, v251\n"
"v_mul_f32 v252, s69, v252\n"
"v_mfma_f32_32x32x16_bf16 v[144:159], a[116:119], a[152:155], v[144:159]\n"
"s_waitcnt lgkmcnt(9)\n"
"global_store_dwordx4 %[vO], v[44:47], s[96:97] offset:0 sc0 sc1\n"
"v_mul_f32 v253, s69, v253\n"
"v_mul_f32 v254, s69, v254\n"
"v_mul_f32 v255, s69, v255\n"
"v_exp_f32 v64, -|v240|\n"
"v_mfma_f32_32x32x16_bf16 v[128:143], a[120:123], a[156:159], v[128:143]\n"
"s_add_u32 s96, s96, 8192\n"
"s_addc_u32 s97, s97, 0\n"
"v_exp_f32 v65, -|v241|\n"
"v_exp_f32 v66, -|v242|\n"
"v_exp_f32 v67, -|v243|\n"
"v_mfma_f32_32x32x16_bf16 v[144:159], a[124:127], a[156:159], v[144:159]\n"
"v_exp_f32 v68, -|v244|\n"
"v_exp_f32 v69, -|v245|\n"
"v_exp_f32 v70, -|v246|\n"
"v_exp_f32 v71, -|v247|\n"
"v_mfma_f32_32x32x16_bf16 v[160:175], a[24:27], a[172:175], v[160:175]\n"
"v_exp_f32 v248, -v248\n"
"v_exp_f32 v249, -v249\n"
"v_exp_f32 v250, -v250\n"
"v_mfma_f32_32x32x16_bf16 v[176:191], a[28:31], a[172:175], v[176:191]\n"
"v_exp_f32 v251, -v251\n"
"v_exp_f32 v252, -v252\n"
"v_exp_f32 v253, -v253\n"
"v_mfma_f32_32x32x16_bf16 v[160:175], a[32:35], a[176:179], v[160:175]\n"
"v_exp_f32 v254, -v254\n"
"v_exp_f32 v255, -v255\n"
"v_add_f32 v72, 1.0, v64\n"
"v_add_f32 v73, 1.0, v65\n"
"v_add_f32 v74, 1.0, v66\n"
"v_mfma_f32_32x32x16_bf16 v[176:191], a[36:39], a[176:179], v[176:191]\n"
"v_add_f32 v75, 1.0, v67\n"
"v_add_f32 v76, 1.0, v68\n"
"v_add_f32 v77, 1.0, v69\n"
"v_add_f32 v78, 1.0, v70\n"
"v_add_f32 v79, 1.0, v71\n"
"v_fmac_f32 v72, v72, v248\n"
"v_cvt_pk_bf16_f32 v128, v128, v129\n"
"v_mfma_f32_32x32x16_bf16 v[160:175], a[40:43], a[180:183], v[160:175]\n"
"v_cvt_pk_bf16_f32 v129, v130, v131\n"
"v_cvt_pk_bf16_f32 v130, v132, v133\n"
"v_cvt_pk_bf16_f32 v131, v134, v135\n"
"v_fmac_f32 v73, v73, v249\n"
"v_fmac_f32 v74, v74, v250\n"
"v_mfma_f32_32x32x16_bf16 v[176:191], a[44:47], a[180:183], v[176:191]\n"
"ds_write_b128 v124, v[128:131]\n"
"v_fmac_f32 v75, v75, v251\n"
"v_fmac_f32 v76, v76, v252\n"
"v_fmac_f32 v77, v77, v253\n"
"v_fmac_f32 v78, v78, v254\n"
"v_fmac_f32 v79, v79, v255\n"
"v_rcp_f32 v72, v72\n"
"v_mfma_f32_32x32x16_bf16 v[160:175], a[48:51], a[184:187], v[160:175]\n"
"v_cvt_pk_bf16_f32 v136, v136, v137\n"
"v_cvt_pk_bf16_f32 v137, v138, v139\n"
"v_cvt_pk_bf16_f32 v138, v140, v141\n"
"v_cvt_pk_bf16_f32 v139, v142, v143\n"
"v_rcp_f32 v73, v73\n"
"v_mfma_f32_32x32x16_bf16 v[176:191], a[52:55], a[184:187], v[176:191]\n"
"ds_write_b128 v125, v[136:139]\n"
"v_rcp_f32 v74, v74\n"
"v_rcp_f32 v75, v75\n"
"v_rcp_f32 v76, v76\n"
"v_mfma_f32_32x32x16_bf16 v[160:175], a[56:59], a[188:191], v[160:175]\n"
"v_rcp_f32 v77, v77\n"
"v_rcp_f32 v78, v78\n"
"v_rcp_f32 v79, v79\n"
"v_fma_f32 v64, -v64, v72, v72\n"
"v_mfma_f32_32x32x16_bf16 v[176:191], a[60:63], a[188:191], v[176:191]\n"
"v_cvt_pk_bf16_f32 v144, v144, v145\n"
"v_cvt_pk_bf16_f32 v145, v146, v147\n"
"v_cvt_pk_bf16_f32 v146, v148, v149\n"
"v_cvt_pk_bf16_f32 v147, v150, v151\n"
"ds_write_b128 v126, v[144:147]\n"
"v_fma_f32 v65, -v65, v73, v73\n"
"v_fma_f32 v66, -v66, v74, v74\n"
"v_fma_f32 v67, -v67, v75, v75\n"
"v_fma_f32 v68, -v68, v76, v76\n"
"v_fma_f32 v69, -v69, v77, v77\n"
"v_fma_f32 v70, -v70, v78, v78\n"
"v_fma_f32 v71, -v71, v79, v79\n"
"v_bfi_b32 v64, s70, v64, v240\n"
"v_cvt_pk_bf16_f32 v152, v152, v153\n"
"v_cvt_pk_bf16_f32 v153, v154, v155\n"
"v_cvt_pk_bf16_f32 v154, v156, v157\n"
"v_cvt_pk_bf16_f32 v155, v158, v159\n"
"ds_write_b128 v127, v[152:155]\n"
"v_bfi_b32 v65, s70, v65, v241\n"
"v_bfi_b32 v66, s70, v66, v242\n"
"v_bfi_b32 v67, s70, v67, v243\n"
"v_bfi_b32 v68, s70, v68, v244\n"
"v_bfi_b32 v69, s70, v69, v245\n"
"v_bfi_b32 v70, s70, v70, v246\n"
"v_bfi_b32 v71, s70, v71, v247\n"
"v_cvt_pk_bf16_f32 v120, v64, v65\n"
"v_cvt_pk_bf16_f32 v121, v66, v67\n"
"v_cvt_pk_bf16_f32 v122, v68, v69\n"
"v_cvt_pk_bf16_f32 v123, v70, v71\n"
"ds_write_b128 %[vZw], v[120:123] offset:19456\n"
"ds_read_b128 v[132:135], %[vZw] offset:0\n"
"ds_read_b128 v[140:143], %[vZw] offset:1024\n"
"ds_read_b128 v[148:151], %[vZw] offset:2048\n"
"ds_read_b128 v[156:159], %[vZw] offset:3072\n"
"s_waitcnt lgkmcnt(4)\n"
"s_barrier\n"
"ds_read_b128 a[128:131], %[vZr] offset:18432\n"
"ds_read_b128 a[132:135], %[vZr] offset:18944\n"
"ds_read_b128 a[136:139], %[vZr] offset:38912\n"
"ds_read_b128 a[140:143], %[vZr] offset:39424\n"
"ds_read_b128 a[144:147], v112 offset:18432\n"
"ds_read_b128 a[148:151], v112 offset:18944\n"
"ds_read_b128 a[152:155], v112 offset:38912\n"
"ds_read_b128 a[156:159], v112 offset:39424\n"
"s_waitcnt lgkmcnt(7)\n"
"v_mfma_f32_32x32x16_bf16 v[160:175], a[64:67], a[128:131], v[160:175]\n"
"s_waitcnt lgkmcnt(6)\n"
"v_mfma_f32_32x32x16_bf16 v[160:175], a[72:75], a[132:135], v[160:175]\n"
"s_waitcnt lgkmcnt(11)\n"
"global_store_dwordx4 %[vO], v[132:135], s[96:97] offset:-4096 sc0 sc1\n"
"s_waitcnt lgkmcnt(5)\n"
"v_mfma_f32_32x32x16_bf16 v[160:175], a[80:83], a[136:139], v[160:175]\n"
"s_waitcnt lgkmcnt(4)\n"
"v_mfma_f32_32x32x16_bf16 v[160:175], a[88:91], a[140:143], v[160:175]\n"
"s_waitcnt lgkmcnt(3)\n"
"v_mfma_f32_32x32x16_bf16 v[160:175], a[96:99], a[144:147], v[160:175]\n"
"s_waitcnt lgkmcnt(10)\n"
"global_store_dwordx4 %[vO], v[140:143], s[96:97] offset:0 sc0 sc1\n"
"s_add_u32 s96, s96, 8192\n"
"s_addc_u32 s97, s97, 0\n"
"s_waitcnt lgkmcnt(2)\n"
"v_mfma_f32_32x32x16_bf16 v[160:175], a[104:107], a[148:151], v[160:175]\n"
"s_waitcnt lgkmcnt(1)\n"
"v_mfma_f32_32x32x16_bf16 v[160:175], a[112:115], a[152:155], v[160:175]\n"
"s_waitcnt lgkmcnt(0)\n"
"v_mfma_f32_32x32x16_bf16 v[160:175], a[120:123], a[156:159], v[160:175]\n"
"s_waitcnt lgkmcnt(9)\n"
"global_store_dwordx4 %[vO], v[148:151], s[96:97] offset:-4096 sc0 sc1\n"
"v_mfma_f32_32x32x16_bf16 v[176:191], a[68:71], a[128:131], v[176:191]\n"
"v_mfma_f32_32x32x16_bf16 v[176:191], a[76:79], a[132:135], v[176:191]\n"
"v_mfma_f32_32x32x16_bf16 v[176:191], a[84:87], a[136:139], v[176:191]\n"
"s_waitcnt lgkmcnt(8)\n"
"global_store_dwordx4 %[vO], v[156:159], s[96:97] offset:0 sc0 sc1\n"
"s_add_u32 s96, s96, 8192\n"
"s_addc_u32 s97, s97, 0\n"
"s_nop 7\n"
"v_mfma_f32_32x32x16_bf16 v[176:191], a[92:95], a[140:143], v[176:191]\n"
"v_cvt_pk_bf16_f32 v160, v160, v161\n"
"v_cvt_pk_bf16_f32 v161, v162, v163\n"
"v_mfma_f32_32x32x16_bf16 v[176:191], a[100:103], a[144:147], v[176:191]\n"
"v_cvt_pk_bf16_f32 v162, v164, v165\n"
"v_cvt_pk_bf16_f32 v163, v166, v167\n"
"v_mfma_f32_32x32x16_bf16 v[176:191], a[108:111], a[148:151], v[176:191]\n"
"v_cvt_pk_bf16_f32 v168, v168, v169\n"
"v_mfma_f32_32x32x16_bf16 v[176:191], a[116:119], a[152:155], v[176:191]\n"
"ds_write_b128 v124, v[160:163]\n"
"v_cvt_pk_bf16_f32 v169, v170, v171\n"
"v_cvt_pk_bf16_f32 v170, v172, v173\n"
"v_mfma_f32_32x32x16_bf16 v[176:191], a[124:127], a[156:159], v[176:191]\n"
"v_cvt_pk_bf16_f32 v171, v174, v175\n"
"ds_write_b128 v125, v[168:171]\n"
"s_nop 11\n"
"v_cvt_pk_bf16_f32 v176, v176, v177\n"
"v_cvt_pk_bf16_f32 v177, v178, v179\n"
"v_cvt_pk_bf16_f32 v178, v180, v181\n"
"v_cvt_pk_bf16_f32 v179, v182, v183\n"
"ds_write_b128 v126, v[176:179]\n"
"v_cvt_pk_bf16_f32 v184, v184, v185\n"
"v_cvt_pk_bf16_f32 v185, v186, v187\n"
"v_cvt_pk_bf16_f32 v186, v188, v189\n"
"v_cvt_pk_bf16_f32 v187, v190, v191\n"
"ds_write_b128 v127, v[184:187]\n"
"ds_read_b128 v[164:167], %[vZw] offset:0\n"
"ds_read_b128 v[172:175], %[vZw] offset:1024\n"
"ds_read_b128 v[180:183], %[vZw] offset:2048\n"
"ds_read_b128 v[188:191], %[vZw] offset:3072\n"
"s_waitcnt lgkmcnt(3)\n"
"global_store_dwordx4 %[vO], v[164:167], s[96:97] offset:-4096 sc0 sc1\n"
"s_waitcnt lgkmcnt(2)\n"
"global_store_dwordx4 %[vO], v[172:175], s[96:97] offset:0 sc0 sc1\n"
"s_add_u32 s96, s96, 8192\n"
"s_addc_u32 s97, s97, 0\n"
"s_waitcnt lgkmcnt(1)\n"
"global_store_dwordx4 %[vO], v[180:183], s[96:97] offset:-4096 sc0 sc1\n"
"s_waitcnt lgkmcnt(0)\n"
"global_store_dwordx4 %[vO], v[188:191], s[96:97] offset:0 sc0 sc1\n"
"s_add_u32 s96, s96, 8192\n"
"s_addc_u32 s97, s97, 0\n"
".Lwn_end:\n"
      :
      : [px] "s"(x), [xbytes] "s"(xbytes), [sx0] "s"(sx0), [sx1] "s"(sx1), [sx4] "s"(sx4), [sm0] "s"(sm0), [sm4] "s"(sm4),
        [pw] "s"(w), [pwo] "s"(wo), [pout] "s"(pout), [ring] "s"(ring), [cb0] "s"(cb0), [spf] "s"(spf),
        [wof0] "s"(tp[0] * 262144), [wof1] "s"(tp[1] * 262144), [wof2] "s"(tp[2] * 262144), [wof3] "s"(tp[3] * 262144),
        [wof4] "s"(tp[4] * 262144),
        [vX] "v"(vX), [vWd] "v"(vWd), [vTA] "v"(vTA), [vTO] "v"(vTO), [vWo] "v"(vWo), [vZr] "v"(vZr),
        [vZw] "v"(vZw), [vO] "v"(vO), [vOw] "v"(vOw),
        [xa0] "v"(lds0 + xpos(ml + 8 - 2 * tp[0], q)), [xa1] "v"(lds0 + xpos(ml + 8 - 2 * tp[1], q)),
        [xa2] "v"(lds0 + xpos(ml + 8 - 2 * tp[2], q)), [xa3] "v"(lds0 + xpos(ml + 8 - 2 * tp[3], q)),
        [xa4] "v"(lds0 + xpos(ml + 8 - 2 * tp[4], q))
      : "v16", "v17", "v18", "v19", "v20", "v21", "v22", "v23", "v24", "v25", "v26", "v27", "v28", "v29", "v30", "v31", "v32", "v33", "v34", "v35", "v36", "v37", "v38", "v39", "v40", "v41", "v42", "v43", "v44", "v45", "v46", "v47", "v48", "v49", "v50", "v51", "v52", "v53", "v54", "v55", "v56", "v57", "v58", "v59", "v60", "v61", "v62", "v63", "v64", "v65", "v66", "v67", "v68", "v69", "v70", "v71", "v72", "v73", "v74", "v75", "v76", "v77", "v78", "v79", "v80", "v81", "v82", "v83", "v84", "v85", "v86", "v87", "v88", "v89", "v90", "v91", "v92", "v93", "v94", "v95", "v96", "v97", "v98", "v99", "v100", "v101", "v102", "v103", "v104", "v105", "v106", "v107", "v108", "v109", "v110", "v111", "v112", "v113", "v114", "v115", "v116", "v117", "v118", "v119", "v120", "v121", "v122", "v123", "v124", "v125", "v126", "v127", "v128", "v129", "v130", "v131", "v132", "v133", "v134", "v135", "v136", "v137", "v138", "v139", "v140", "v141", "v142", "v143", "v144", "v145", "v146", "v147", "v148", "v149", "v150", "v151", "v152", "v153", "v154", "v155", "v156", "v157", "v158", "v159", "v160", "v161", "v162", "v163", "v164", "v165", "v166", "v167", "v168", "v169", "v170", "v171", "v172", "v173", "v174", "v175", "v176", "v177", "v178", "v179", "v180", "v181", "v182", "v183", "v184", "v185", "v186", "v187", "v188", "v189", "v190", "v191", "v192", "v193", "v194", "v195", "v196", "v197", "v198", "v199", "v200", "v201", "v202", "v203", "v204", "v205", "v206", "v207", "v208", "v209", "v210", "v211", "v212", "v213", "v214", "v215", "v216", "v217", "v218", "v219", "v220", "v221", "v222", "v223", "v224", "v225", "v226", "v227", "v228", "v229", "v230", "v231", "v232", "v233", "v234", "v235", "v236", "v237", "v238", "v239", "v240", "v241", "v242", "v243", "v244", "v245", "v246", "v247", "v248", "v249", "v250", "v251", "v252", "v253", "v254", "v255", "a0", "a1", "a2", "a3", "a4", "a5", "a6", "a7", "a8", "a9", "a10", "a11", "a12", "a13", "a14", "a15", "a16", "a17", "a18", "a19", "a20", "a21", "a22", "a23", "a24", "a25", "a26", "a27", "a28", "a29", "a30", "a31", "a32", "a33", "a34", "a35", "a36", "a37", "a38", "a39", "a40", "a41", "a42", "a43", "a44", "a45", "a46", "a47", "a48", "a49", "a50", "a51", "a52", "a53", "a54", "a55", "a56", "a57", "a58", "a59", "a60", "a61", "a62", "a63", "a64", "a65", "a66", "a67", "a68", "a69", "a70", "a71", "a72", "a73", "a74", "a75", "a76", "a77", "a78", "a79", "a80", "a81", "a82", "a83", "a84", "a85", "a86", "a87", "a88", "a89", "a90", "a91", "a92", "a93", "a94", "a95", "a96", "a97", "a98", "a99", "a100", "a101", "a102", "a103", "a104", "a105", "a106", "a107", "a108", "a109", "a110", "a111", "a112", "a113", "a114", "a115", "a116", "a117", "a118", "a119", "a120", "a121", "a122", "a123", "a124", "a125", "a126", "a127", "a128", "a129", "a130", "a131", "a132", "a133", "a134", "a135", "a136", "a137", "a138", "a139", "a140", "a141", "a142", "a143", "a144", "a145", "a146", "a147", "a148", "a149", "a150", "a151", "a152", "a153", "a154", "a155", "a156", "a157", "a158", "a159", "a160", "a161", "a162", "a163", "a164", "a165", "a166", "a167", "a168", "a169", "a170", "a171", "a172", "a173", "a174", "a175", "a176", "a177", "a178", "a179", "a180", "a181", "a182", "a183", "a184", "a185", "a186", "a187", "a188", "a189", "a190", "a191", "a192", "a193", "a194", "a195", "a196", "a197", "a198", "a199", "a200", "a201", "a202", "a203", "a204", "a205", "a206", "a207", "a208", "a209", "a210", "a211", "a212", "a213", "a214", "a215", "a216", "a217", "a218", "a219", "a220", "a221", "a222", "a223", "a224", "a225", "a226", "a227", "a228", "a229", "a230", "a231", "a232", "a233", "a234", "a235", "a236", "a237", "a238", "a239", "a240", "a241", "a242", "a243", "a244", "a245", "a246", "a247", "a248", "a249", "a250", "a251", "a252", "a253", "a254", "a255", "s39", "s40", "s41", "s42", "s43", "s44", "s45", "s46", "s47", "s48", "s49", "s50", "s51", "s52", "s53", "s54", "s55", "s56", "s57", "s58", "s59", "s60", "s61", "s62", "s63", "s64", "s65", "s66", "s67", "s68", "s69", "s70", "s71", "s72", "s73", "s74", "s75", "s76", "s77", "s78", "s79", "s80", "s81", "s82", "s83", "s84", "s85", "s86", "s87", "s88", "s89", "s90", "s91", "s92", "s93", "s94", "s95", "s96", "s97", "s98", "s99", "vcc", "scc", "memory");
}

extern "C" void kernel_launch(void* const* d_in, const int* in_sizes, int n_in, void* d_out, int out_size,
                              void* d_ws, size_t ws_size, hipStream_t stream) {
  int rows = in_sizes[0] / kD;
  wavenet_kernel<<<rows / kBM, 256, 0, stream>>>((const char*)d_in[0], (const char*)d_in[1], (const char*)d_in[2],
                                                 (char*)d_out, (char*)d_ws, (unsigned)in_sizes[0] * 2u);
}
```
